```python
import torch

def _softmax(x, axis=-1):
    return torch.softmax(x, dim=axis)
def _gelu(x):
    return torch.nn.functional.gelu(x, approximate="tanh")
def _silu(x):
    return torch.nn.functional.silu(x)
def _sigmoid(x):
    return torch.sigmoid(x)
def _layernorm(x, axis=-1, eps=1e-5):
    m = x.mean(dim=axis, keepdim=True)
    v = x.var(dim=axis, keepdim=True, unbiased=False)
    return (x - m) / torch.sqrt(v + eps)
def _scatter_add(tgt, idx, src, axis=0):
    return tgt.clone().index_add_(axis, idx, src)
def _segment_sum(src, seg, n):
    out = torch.zeros((n,) + src.shape[1:], dtype=src.dtype)
    return out.index_add_(0, seg, src)
def _pad(x, pw):
    flat = tuple(v for pair in reversed(pw) for v in pair)
    return torch.nn.functional.pad(x, flat)

kernel_name = 'gated_mix__mha_block__grouped_kv_mqa__prompt'

def setup_inputs():

    _w = (lambda a: (lambda t: t.to(torch.bfloat16) if t.is_floating_point() else t)(torch.as_tensor(a)))
    def _A_setup():
        B, S, D, H = 1, 2048, 640, 32
        return {
            "x": _w((lambda *s: torch.randn(*s, dtype=torch.bfloat16) * 0.1)(B, S, D)),
            "wqkv": _w((lambda *s: torch.randn(*s, dtype=torch.bfloat16) * 0.4)(D, 3 * D)),
            "wo": _w((lambda *s: torch.randn(*s, dtype=torch.bfloat16) * 0.2)(D, D)),
        }
    _A_in = _A_setup()

    def _B_setup():
        B, S, D, H = 1, 2048, 640, 32
        Dh = D // H
        return {
            "x": _w((lambda *s: torch.randn(*s, dtype=torch.bfloat16) * 0.1)(B, S, D)),
            "wq": _w((lambda *s: torch.randn(*s, dtype=torch.bfloat16) * 0.4)(D, H * Dh)),
            "wk": _w((lambda *s: torch.randn(*s, dtype=torch.bfloat16) * 0.4)(D, Dh)),
            "wv": _w((lambda *s: torch.randn(*s, dtype=torch.bfloat16) * 0.4)(D, Dh)),
            "wo": _w((lambda *s: torch.randn(*s, dtype=torch.bfloat16) * 0.4)(H * Dh, D)),
        }
    _B_in = _B_setup()

    return {
        "A_x": _A_in["x"],
        "A_wqkv": _A_in["wqkv"],
        "A_wo": _A_in["wo"],
        "B_x": _B_in["x"],
        "B_wq": _B_in["wq"],
        "B_wk": _B_in["wk"],
        "B_wv": _B_in["wv"],
        "B_wo": _B_in["wo"],
        "gate_w": _w((lambda *s: torch.randn(*s, dtype=torch.bfloat16) * 0.1)(640) * 16.0),
    }

def reference(A_x, A_wqkv, A_wo, B_x, B_wq, B_wk, B_wv, B_wo, gate_w):
    def _A_ref(x, wqkv, wo):
        B, S, D = x.shape
        H = 32
        Dh = D // H
        qkv = (lambda x, *d: x.permute(*d))((x @ wqkv).reshape(B, S, 3, H, Dh), 2, 0, 3, 1, 4)
        q, k, v = qkv[0], qkv[1], qkv[2]
        scores = torch.einsum("bhqd,bhkd->bhqk", q, k) * (Dh ** -0.5)
        p = _softmax(scores, axis=-1)
        o = torch.einsum("bhqk,bhkd->bhqd", p, v)
        return (lambda x, *d: x.permute(*d))(o, 0, 2, 1, 3).reshape(B, S, D) @ wo

    def _B_ref(x, wq, wk, wv, wo):
        B, S, D = x.shape
        H = 32
        Dh = D // H
        q = (lambda x, *d: x.permute(*d))((x @ wq).reshape(B, S, H, Dh), 0, 2, 1, 3)
        k = x @ wk
        v = x @ wv
        scores = torch.einsum("bhqd,bkd->bhqk", q, k) * (Dh ** -0.5)
        p = _softmax(scores, axis=-1)
        o = torch.einsum("bhqk,bkd->bhqd", p, v)
        return (lambda x, *d: x.permute(*d))(o, 0, 2, 1, 3).reshape(B, S, H * Dh) @ wo

    x = A_x
    g = _sigmoid((x * gate_w).sum(-1, keepdims=True))
    a = _A_ref(x, A_wqkv, A_wo)
    b = _B_ref(x, B_wq, B_wk, B_wv, B_wo)
    return g * a + (1 - g) * b
```

```cpp
#include <hip/hip_runtime.h>
#include <hip/hip_bf16.h>
#include <stdint.h>

typedef __attribute__((ext_vector_type(8))) short bf16x8;
typedef __attribute__((ext_vector_type(4))) short bf16x4;
typedef __attribute__((ext_vector_type(16))) float f32x16;
typedef __attribute__((ext_vector_type(4))) float f32x4;
typedef __attribute__((ext_vector_type(2))) float f32x2;
typedef __attribute__((ext_vector_type(4))) unsigned u32x4;
typedef __attribute__((ext_vector_type(2))) unsigned u32x2;
typedef __attribute__((ext_vector_type(2))) __bf16 bf16x2_t;
typedef __attribute__((address_space(3))) const char* lds_cptr;
typedef unsigned short u16;

#define DI __device__ __forceinline__
#define SBAR() __builtin_amdgcn_sched_barrier(0)
#define PIN(x) asm volatile("" : "+v"(x))
#define MFMA32(a, b, c) __builtin_amdgcn_mfma_f32_32x32x16_bf16((a), (b), (c), 0, 0, 0)
#define MFMA8(a, b, c) __builtin_amdgcn_mfma_f32_32x32x8bf16_1k((a), (b), (c), 0, 0, 0)
#define MFMA16(a, b, c) __builtin_amdgcn_mfma_f32_16x16x32_bf16((a), (b), (c), 0, 0, 0)

constexpr int S = 2048, D = 640;
constexpr float QSCALE = 0.22360679774997896f * 1.4426950408889634f;

constexpr size_t HEAD_B = (size_t)S * 48;
constexpr size_t WS_Q = 0;
constexpr size_t WS_K = WS_Q + 64 * HEAD_B;
constexpr size_t WS_V = WS_K + 33 * HEAD_B;
constexpr size_t WS_G = WS_V + 33 * HEAD_B;
constexpr size_t WS_O = WS_G + S * 4;
constexpr size_t WS_W = WS_O + (size_t)160 * S * 16;
constexpr size_t WS_END = WS_W + (size_t)160 * D * 16;

DI unsigned cvtpk(float lo, float hi) {
  f32x2 v = {lo, hi};
  return __builtin_bit_cast(unsigned, __builtin_convertvector(v, bf16x2_t));
}
DI void glds16(const void* sbase, unsigned voff, unsigned lds_dst) {
  unsigned keep;
  asm volatile("s_mov_b32 %0, m0\n\ts_mov_b32 m0, %3\n\ts_nop 0\n\tglobal_load_lds_dwordx4 %1, %2\n\ts_mov_b32 m0, %0"
               : "=&s"(keep) : "v"(voff), "s"(sbase), "s"(lds_dst) : "memory");
}
DI bf16x8 lds_b128(lds_cptr p) { return *(const __attribute__((address_space(3))) bf16x8*)p; }
DI bf16x4 lds_b64(lds_cptr p) { return *(const __attribute__((address_space(3))) bf16x4*)p; }
DI bf16x4 lds_tr(lds_cptr p) {
  return __builtin_amdgcn_ds_read_tr16_b64_v4i16((__attribute__((address_space(3))) bf16x4*)p);
}
DI bf16x8 cat4(bf16x4 a, bf16x4 b) { return __builtin_shufflevector(a, b, 0, 1, 2, 3, 4, 5, 6, 7); }
#define WAIT_BAR(N) asm volatile("s_waitcnt vmcnt(" #N ") lgkmcnt(0)\n\ts_barrier" ::: "memory")

constexpr int P_XB = 16384, P_WB = 20480, P_EB = 3072, P_STAGE = P_XB + P_WB + P_EB, P_NST = 4;
constexpr int P_GATE = P_STAGE * P_NST, P_LDS = P_GATE + 1280;

__global__ __launch_bounds__(512, 1) void k1_proj(const u16* __restrict__ x, const u16* __restrict__ wqkv, const u16* __restrict__ wq,
                                                  const u16* __restrict__ wk, const u16* __restrict__ wv, const u16* __restrict__ gw,
                                                  char* __restrict__ ws) {
  __shared__ __attribute__((aligned(16))) char lds[P_LDS];
  const int tid = threadIdx.x, lane = tid & 63, r = lane & 31, h = lane >> 5;
  const int wid = __builtin_amdgcn_readfirstlane(tid >> 6);
  const int rt = wid & 3, grp = wid >> 2;
  const int xcd = blockIdx.x & 7, bi = blockIdx.x >> 3;
  const int mt = (xcd & 1) * 8 + (bi & 7), j = (xcd >> 1) * 4 + (bi >> 3);
  const int m0 = mt * 128;
  const int typ = j >> 2;
  const u16* wsrc = (typ < 3) ? (wqkv + j * 160) : (wq + (j - 12) * 160);
  const unsigned ldw = (typ < 3) ? 3840u : 1280u;
  const bool wg_extra = (j & 1) == 0;
  const int ex_rt = (j >> 1) & 3, ex_typ = j >> 3;
  const bool my_extra = wg_extra && grp == 1 && rt == ex_rt;
  const u16* esrc = ex_typ ? wv : wk;
  const unsigned lds0 = (unsigned)(uintptr_t)lds;

  unsigned xoff[2], woff[3];
#pragma unroll
  for (int i = 0; i < 2; ++i) {
    const int ins = wid * 2 + i, row = ins * 8 + (lane >> 3), c = (lane & 7) ^ ((row >> 1) & 7);
    xoff[i] = (unsigned)(m0 + row) * 1280u + c * 16;
  }
  const int wins0 = grp == 0 ? wid * 3 : 12 + (wid - 4) * 2;
#pragma unroll
  for (int i = 0; i < 3; ++i) {
    const int idx = (wins0 + i) * 64 + lane, row = idx / 20, gr = idx - row * 20;
    woff[i] = (unsigned)row * ldw + gr * 16;
  }
  auto stage = [&](int s) {
    const unsigned dst = lds0 + (s & 3) * P_STAGE;
    const char* xb = (const char*)x + s * 128;
    const char* wb = (const char*)wsrc + (size_t)s * 64 * ldw;
    glds16(xb, xoff[0], dst + (wid * 2) * 1024);
    glds16(xb, xoff[1], dst + (wid * 2 + 1) * 1024);
    glds16(wb, woff[0], dst + P_XB + wins0 * 1024);
    glds16(wb, woff[1], dst + P_XB + (wins0 + 1) * 1024);
    if (grp == 0) glds16(wb, woff[2], dst + P_XB + (wins0 + 2) * 1024);
    else if (wg_extra && rt < 3) {
      if (rt < 2 || lane < 32) glds16((const char*)esrc + s * 2560 + rt * 1024, lane * 16, dst + P_XB + P_WB + rt * 1024);
    }
  };
  stage(0); stage(1); stage(2);
  if (tid < 80) *(u32x4*)(lds + P_GATE + tid * 16) = *(const u32x4*)((const char*)gw + tid * 16);

  const int xrow = rt * 32 + r, xsw = h ^ ((xrow >> 1) & 7);
  const lds_cptr L = (lds_cptr)lds;
  const int q4 = (lane & 15) >> 2, p4 = lane & 3, b16 = (lane >> 4) & 1;
  const int wbase = P_XB + (8 * h + q4) * 320 + (16 * b16 + 4 * p4) * 2 + grp * 192;
  const int ebase = P_XB + P_WB + (8 * h + q4) * 40 + (16 * b16 + 4 * p4) * 2;
  f32x16 acc0 = {}, acc1 = {}, acc2 = {};
  const bool vtype = typ == 2;

#pragma unroll 1
  for (int s = 0; s < 10; ++s) {
    if (s <= 7) WAIT_BAR(8); else if (s == 8) WAIT_BAR(4); else WAIT_BAR(0);
    if (s + 3 < 10) stage(s + 3);
    const lds_cptr st = L + (s & 3) * P_STAGE;
#pragma unroll
    for (int ks = 0; ks < 4; ++ks) {
      const bf16x8 xf = lds_b128(st + xrow * 128 + ((2 * ks) ^ xsw) * 16);
      const lds_cptr wp = st + wbase + ks * 16 * 320;
      const bf16x8 w0 = cat4(lds_tr(wp), lds_tr(wp + 4 * 320));
      const bf16x8 w1 = cat4(lds_tr(wp + 64), lds_tr(wp + 64 + 4 * 320));
      if (vtype) { acc0 = MFMA32(xf, w0, acc0); acc1 = MFMA32(xf, w1, acc1); }
      else       { acc0 = MFMA32(w0, xf, acc0); acc1 = MFMA32(w1, xf, acc1); }
      if (grp == 0) {
        const bf16x8 w2 = cat4(lds_tr(wp + 128), lds_tr(wp + 128 + 4 * 320));
        if (vtype) acc2 = MFMA32(xf, w2, acc2); else acc2 = MFMA32(w2, xf, acc2);
      } else if (my_extra) {
        const lds_cptr ep = st + ebase + ks * 16 * 40;
        bf16x8 w2 = cat4(lds_tr(ep), lds_tr(ep + 4 * 40));
        if (ex_typ == 0) {
          const bf16x8 gf = lds_b128(L + P_GATE + (s * 64 + ks * 16 + 8 * h) * 2);
          if (r == 20) w2 = gf;
          acc2 = MFMA32(w2, xf, acc2);
        } else {
          acc2 = MFMA32(xf, w2, acc2);
        }
      }
    }
  }

  const int tok = m0 + rt * 32 + r;
  const int ntile = grp == 0 ? 3 : 2;
  if (!vtype) {
    char* base = ws + (typ == 1 ? WS_K + (size_t)(j - 4) * 8 * HEAD_B : WS_Q + (size_t)(typ == 0 ? j : j - 8) * 8 * HEAD_B) + (size_t)tok * 48;
    const float sc = typ == 1 ? 1.0f : QSCALE;
#pragma unroll
    for (int i = 0; i < 3; ++i) {
      if (i < ntile) {
        const f32x16& a = i == 0 ? acc0 : (i == 1 ? acc1 : acc2);
#pragma unroll
        for (int g = 0; g < 4; ++g) {
          const int qd = 8 * (grp * 3 + i) + 2 * g + h, hl = (qd * 205) >> 10, dq = qd - hl * 5;
          u32x2 v = {cvtpk(a[4 * g] * sc, a[4 * g + 1] * sc), cvtpk(a[4 * g + 2] * sc, a[4 * g + 3] * sc)};
          *(u32x2*)(base + (size_t)hl * HEAD_B + dq * 8) = v;
        }
      }
    }
    if (typ == 1 && grp == 1) {
#pragma unroll
      for (int i = 0; i < 4; ++i) *(u32x2*)(base + (size_t)(2 * i + h) * HEAD_B + 40) = (u32x2){0x3F80u, 0u};
    }
  } else {
    char* base = ws + WS_V + (size_t)(j - 8) * 8 * HEAD_B + (size_t)((m0 + rt * 32) / 16) * 768 + h * 384;
#pragma unroll
    for (int i = 0; i < 3; ++i) {
      if (i < ntile) {
        const f32x16& a = i == 0 ? acc0 : (i == 1 ? acc1 : acc2);
        const int n = 32 * (grp * 3 + i) + r, hl = (n * 205) >> 12, d = n - hl * 20;
#pragma unroll
        for (int sp = 0; sp < 2; ++sp) {
          u32x4 v = {cvtpk(a[8 * sp], a[8 * sp + 1]), cvtpk(a[8 * sp + 2], a[8 * sp + 3]),
                     cvtpk(a[8 * sp + 4], a[8 * sp + 5]), cvtpk(a[8 * sp + 6], a[8 * sp + 7])};
          *(u32x4*)(base + (size_t)hl * HEAD_B + sp * 768 + d * 16) = v;
        }
      }
    }
    if (grp == 1) {
      const unsigned cv = (r & 3) == 0 ? 0x3F803F80u : 0u;
#pragma unroll
      for (int sp = 0; sp < 2; ++sp) *(u32x4*)(base + (size_t)(r >> 2) * HEAD_B + sp * 768 + (20 + (r & 3)) * 16) = (u32x4){cv, cv, cv, cv};
    }
  }
  if (my_extra) {
    if (ex_typ == 0) {
      char* base = ws + WS_K + (size_t)32 * HEAD_B + (size_t)tok * 48;
#pragma unroll
      for (int g = 0; g < 3; ++g) {
        u32x2 v = {cvtpk(acc2[4 * g], acc2[4 * g + 1]), cvtpk(acc2[4 * g + 2], acc2[4 * g + 3])};
        if (g == 2 && h == 1) v = (u32x2){0x3F80u, 0u};
        *(u32x2*)(base + (2 * g + h) * 8) = v;
      }
      if (h == 1) *(float*)(ws + WS_G + (size_t)tok * 4) = 1.0f / (1.0f + __expf(-acc2[8]));
    } else if (r < 24) {
      char* base = ws + WS_V + (size_t)32 * HEAD_B + (size_t)((m0 + rt * 32) / 16) * 768 + h * 384 + r * 16;
      const unsigned cv = r == 20 ? 0x3F803F80u : 0u;
#pragma unroll
      for (int sp = 0; sp < 2; ++sp) {
        u32x4 v = {cvtpk(acc2[8 * sp], acc2[8 * sp + 1]), cvtpk(acc2[8 * sp + 2], acc2[8 * sp + 3]),
                   cvtpk(acc2[8 * sp + 4], acc2[8 * sp + 5]), cvtpk(acc2[8 * sp + 6], acc2[8 * sp + 7])};
        if (r >= 20) v = (u32x4){cv, cv, cv, cv};
        *(u32x4*)(base + sp * 768) = v;
      }
    }
  }
}

constexpr int A_CHUNK = 24576, A_KB = 12288, A_NSLOT = 3, A_LDS = A_CHUNK * A_NSLOT + 1024;

DI float half_max(float m) {
  auto rr = __builtin_amdgcn_permlane32_swap(__float_as_uint(m), __float_as_uint(m), false, false);
  return fmaxf(__uint_as_float(rr[0]), __uint_as_float(rr[1]));
}
DI float max16(const f32x16& s) {
  float a = fmaxf(fmaxf(s[0], s[1]), s[2]), b = fmaxf(fmaxf(s[3], s[4]), s[5]);
  a = fmaxf(fmaxf(a, s[6]), s[7]); b = fmaxf(fmaxf(b, s[8]), s[9]);
  a = fmaxf(fmaxf(a, s[10]), s[11]); b = fmaxf(fmaxf(b, s[12]), s[13]);
  a = fmaxf(fmaxf(a, s[14]), s[15]);
  return half_max(fmaxf(a, b));
}
DI f32x16 attn_safe(const char* kg, const char* vg, bf16x8 q1, bf16x4 q2, int r, int h) {
  if (h) q2 = (bf16x4){0, 0, 0, 0};
  f32x16 o = {};
  float m = -INFINITY;
#pragma unroll 1
  for (int t = 0; t < 64; ++t) {
    const bf16x8 k1 = *(const bf16x8*)(kg + (t * 32 + r) * 48 + 16 * h);
    const bf16x4 k2 = *(const bf16x4*)(kg + (t * 32 + r) * 48 + 32 + 8 * h);
    const int vr = r < 24 ? r : 23;
    const bf16x8 v0 = *(const bf16x8*)(vg + t * 1536 + h * 384 + vr * 16);
    const bf16x8 v1 = *(const bf16x8*)(vg + t * 1536 + 768 + h * 384 + vr * 16);
    f32x16 s = {};
    s = MFMA32(k1, q1, s);
    s = MFMA8(k2, q2, s);
    const float mn = fmaxf(m, max16(s));
    const float al = __builtin_amdgcn_exp2f(m - mn);
    m = mn;
#pragma unroll
    for (int i = 0; i < 16; ++i) { o[i] *= al; s[i] = __builtin_amdgcn_exp2f(s[i] - mn); }
    u32x4 p0 = {cvtpk(s[0], s[1]), cvtpk(s[2], s[3]), cvtpk(s[4], s[5]), cvtpk(s[6], s[7])};
    u32x4 p1 = {cvtpk(s[8], s[9]), cvtpk(s[10], s[11]), cvtpk(s[12], s[13]), cvtpk(s[14], s[15])};
    o = MFMA32(v0, __builtin_bit_cast(bf16x8, p0), o);
    o = MFMA32(v1, __builtin_bit_cast(bf16x8, p1), o);
  }
  return o;
}
DI bool o_unsafe(const f32x16& o, int h) {
  float a = 0.f;
#pragma unroll
  for (int i = 0; i < 8; ++i) a = fmaxf(a, fabsf(o[i]));
  bool nan = false;
#pragma unroll
  for (int i = 0; i < 8; ++i) nan |= (o[i] != o[i]);
  if (h == 0) {
#pragma unroll
    for (int i = 8; i < 12; ++i) { a = fmaxf(a, fabsf(o[i])); nan |= (o[i] != o[i]); }
  }
  bool bad = nan || !(a < 3.0e38f);
  if (h == 1) bad = bad || !(o[8] < 1.0e18f) || !(o[8] > 1.0e-18f);
  return bad;
}
DI void attn_store(const f32x16& o, char* og, int hh, int tok, float gfac, int lane, int h) {
  const float l = __shfl(o[8], lane | 32);
  const float sc = gfac / l;
  u32x4 v = {cvtpk(o[0] * sc, o[1] * sc), cvtpk(o[2] * sc, o[3] * sc), cvtpk(o[4] * sc, o[5] * sc), cvtpk(o[6] * sc, o[7] * sc)};
  *(u32x4*)(og + ((size_t)(hh * 2 + h) * S + tok) * 16) = v;
  if (h == 0) {
    u32x2 t = {cvtpk(o[8] * sc, o[9] * sc), cvtpk(o[10] * sc, o[11] * sc)};
    *(u32x2*)(og + ((size_t)(128 + (hh >> 1)) * S + tok) * 16 + (hh & 1) * 8) = t;
  }
}

__global__ __launch_bounds__(512, 1) void k2_attn(const u16* __restrict__ woa, const u16* __restrict__ wob, char* __restrict__ ws) {
  __shared__ __attribute__((aligned(16))) char lds[A_LDS];
  const int tid = threadIdx.x, lane = tid & 63, r = lane & 31, h = lane >> 5;
  const int wid = __builtin_amdgcn_readfirstlane(tid >> 6);
  const int xcd = blockIdx.x & 7, bi = blockIdx.x >> 3;
  const int hh = (bi >> 4) * 32 + xcd * 4 + ((bi >> 2) & 3), qb = bi & 3;
  const int kvh = hh < 32 ? hh : 32;
  const char* kg = ws + WS_K + (size_t)kvh * HEAD_B;
  const char* vg = ws + WS_V + (size_t)kvh * HEAD_B;
  const int tokA = qb * 512 + wid * 64 + r, tokB = tokA + 32;
  const unsigned lds0 = (unsigned)(uintptr_t)lds;
  const lds_cptr L = (lds_cptr)lds;

  const char* qg = ws + WS_Q + (size_t)hh * HEAD_B;
  const bf16x8 qA1 = *(const bf16x8*)(qg + tokA * 48 + 16 * h), qB1 = *(const bf16x8*)(qg + tokB * 48 + 16 * h);
  bf16x4 qA2 = *(const bf16x4*)(qg + tokA * 48 + 32), qB2 = *(const bf16x4*)(qg + tokB * 48 + 32);

  auto dma = [&](int c, int slot) {
#pragma unroll
    for (int i = 0; i < 3; ++i) {
      const int pc = wid * 3 + i;
      const char* src = pc < 12 ? kg + c * A_KB + pc * 1024 : vg + c * A_KB + (pc - 12) * 1024;
      glds16(src, lane * 16, lds0 + slot * A_CHUNK + pc * 1024);
    }
  };
  dma(0, 0);
  u16 wv8[8];
  const int task = blockIdx.x * 400 + tid;
  if (tid < 400) {
    const int gr = task / 640, n = task - gr * 640;
#pragma unroll
    for (int jj = 0; jj < 8; ++jj) {
      const int hd = gr < 128 ? (gr >> 1) : (2 * (gr - 128) + (jj >> 2));
      const int d = gr < 128 ? (8 * (jj >> 2) + 4 * (gr & 1) + (jj & 3)) : (16 + (jj & 3));
      const u16* w = hd < 32 ? woa + (hd * 20 + d) * 640 : wob + ((hd - 32) * 20 + d) * 640;
      wv8[jj] = w[n];
    }
  }
  dma(1, 1);
  const float gA = *(const float*)(ws + WS_G + tokA * 4), gB = *(const float*)(ws + WS_G + tokB * 4);
  WAIT_BAR(3);

  const int kb1 = r * 48 + 16 * h, kb2 = r * 48 + 32 + 8 * h, vb = A_KB + h * 384 + r * 16;
  bf16x8 k1a = lds_b128(L + kb1), k1b;
  bf16x4 k2a = lds_b64(L + kb2), k2b;
  bf16x8 v0a = {}, v1a = {}, v0b = {}, v1b = {};
  const bf16x4 q2keepA = qA2, q2keepB = qB2;
  f32x16 sA, sB, oA = {}, oB = {};
  const f32x16 zero = {};
  {
    if (h) { qA2 = (bf16x4){0, 0, 0, 0}; qB2 = (bf16x4){0, 0, 0, 0}; }
    sA = MFMA32(k1a, qA1, zero); sB = MFMA32(k1a, qB1, zero);
    sA = MFMA8(k2a, qA2, sA);    sB = MFMA8(k2a, qB2, sB);
    const unsigned ma = cvtpk(-max16(sA), 0.f), mb = cvtpk(-max16(sB), 0.f);
    if (h) { qA2 = __builtin_bit_cast(bf16x4, (u32x2){ma, 0u}); qB2 = __builtin_bit_cast(bf16x4, (u32x2){mb, 0u}); }
    sA = MFMA32(k1a, qA1, zero);
    sA = MFMA8(k2a, qA2, sA);
  }
  if (tid < 400) {
    u32x4 v = {wv8[0] | ((unsigned)wv8[1] << 16), wv8[2] | ((unsigned)wv8[3] << 16), wv8[4] | ((unsigned)wv8[5] << 16), wv8[6] | ((unsigned)wv8[7] << 16)};
    *(u32x4*)(ws + WS_W + (size_t)task * 16) = v;
  }
  u32x4 pA0 = {}, pA1 = {}, pB0 = {}, pB1 = {};

#define EX(v) __builtin_amdgcn_exp2f(v)
#define GAP(MF, X, i, P, e) do { MF; X[i] = EX(X[i]); X[i + 1] = EX(X[i + 1]); X[i + 2] = EX(X[i + 2]); X[i + 3] = EX(X[i + 3]); \
    P[e] = cvtpk(X[i], X[i + 1]); P[e + 1] = cvtpk(X[i + 2], X[i + 3]); PIN(P); SBAR(); } while (0)
#define BF(p) __builtin_bit_cast(bf16x8, p)
#define PHASE(X, PX0, PX1, Y, OY, PY0, PY1, QY1, QY2, K1, K2, V0, V1) do { \
    GAP(Y = MFMA32(K1, QY1, zero), X, 0, PX0, 0);  \
    GAP(Y = MFMA8(K2, QY2, Y),     X, 4, PX0, 2);  \
    GAP(OY = MFMA32(V0, BF(PY0), OY), X, 8, PX1, 0);  \
    GAP(OY = MFMA32(V1, BF(PY1), OY), X, 12, PX1, 2); } while (0)
#define TILE(KP, VP, K1C, K2C, V0C, V1C, K1N, K2N, V0N, V1N) do { \
    K1N = lds_b128((KP) + kb1); K2N = lds_b64((KP) + kb2); V0N = lds_b128((VP) + vb); V1N = lds_b128((VP) + vb + 768); SBAR(); \
    PHASE(sA, pA0, pA1, sB, oB, pB0, pB1, qB1, qB2, K1C, K2C, V0C, V1C); \
    PHASE(sB, pB0, pB1, sA, oA, pA0, pA1, qA1, qA2, K1N, K2N, V0N, V1N); } while (0)

  int slot = 0;
#pragma unroll 1
  for (int c = 0; c < 8; ++c) {
    const int nslot = slot == A_NSLOT - 1 ? 0 : slot + 1;
    const lds_cptr cp = L + slot * A_CHUNK, np = L + nslot * A_CHUNK;
    TILE(cp + 1 * 1536, cp + 0 * 1536, k1a, k2a, v0a, v1a, k1b, k2b, v0b, v1b);
    TILE(cp + 2 * 1536, cp + 1 * 1536, k1b, k2b, v0b, v1b, k1a, k2a, v0a, v1a);
    TILE(cp + 3 * 1536, cp + 2 * 1536, k1a, k2a, v0a, v1a, k1b, k2b, v0b, v1b);
    TILE(cp + 4 * 1536, cp + 3 * 1536, k1b, k2b, v0b, v1b, k1a, k2a, v0a, v1a);
    TILE(cp + 5 * 1536, cp + 4 * 1536, k1a, k2a, v0a, v1a, k1b, k2b, v0b, v1b);
    TILE(cp + 6 * 1536, cp + 5 * 1536, k1b, k2b, v0b, v1b, k1a, k2a, v0a, v1a);
    TILE(cp + 7 * 1536, cp + 6 * 1536, k1a, k2a, v0a, v1a, k1b, k2b, v0b, v1b);
    WAIT_BAR(0);
    if (c + 2 < 8) dma(c + 2, nslot == A_NSLOT - 1 ? 0 : nslot + 1);
    TILE(np, cp + 7 * 1536, k1b, k2b, v0b, v1b, k1a, k2a, v0a, v1a);
    slot = nslot;
  }
  oB = MFMA32(v0a, BF(pB0), oB);
  oB = MFMA32(v1a, BF(pB1), oB);

  if (__builtin_expect(__any(o_unsafe(oA, h) || o_unsafe(oB, h)), 0)) {
    oA = attn_safe(kg, vg, qA1, q2keepA, r, h);
    oB = attn_safe(kg, vg, qB1, q2keepB, r, h);
  }
  char* og = ws + WS_O;
  attn_store(oA, og, hh, tokA, hh < 32 ? gA : 1.0f - gA, lane, h);
  attn_store(oB, og, hh, tokB, hh < 32 ? gB : 1.0f - gB, lane, h);
}

constexpr int O_ROW = 336, O_WAVE = 64 * O_ROW, O_LDS = 4 * O_WAVE;

__global__ __launch_bounds__(256, 1) void k3_out(const char* __restrict__ ws, u16* __restrict__ out) {
  __shared__ __attribute__((aligned(16))) char lds[O_LDS];
  const int tid = threadIdx.x, lane = tid & 63, c16 = lane & 15, g = lane >> 4;
  const int wid = __builtin_amdgcn_readfirstlane(tid >> 6);
  const int xcd = blockIdx.x & 7, bi = blockIdx.x >> 3;
  const int mt = (xcd >> 1) * 8 + (bi & 7), nb = (xcd & 1) * 4 + (bi >> 3);
  const char* og = ws + WS_O + ((size_t)(wid * 40 + g) * S + mt * 64 + c16) * 16;
  const char* wg = ws + WS_W + ((size_t)(wid * 40 + g) * D + nb * 80 + c16) * 16;
  f32x4 acc[4][5] = {};
#pragma unroll
  for (int s = 0; s < 10; ++s) {
    bf16x8 of[4], wf[5];
#pragma unroll
    for (int m = 0; m < 4; ++m) of[m] = *(const bf16x8*)(og + (size_t)s * 4 * S * 16 + m * 256);
#pragma unroll
    for (int n = 0; n < 5; ++n) wf[n] = *(const bf16x8*)(wg + (size_t)s * 4 * D * 16 + n * 256);
#pragma unroll
    for (int m = 0; m < 4; ++m)
#pragma unroll
      for (int n = 0; n < 5; ++n) acc[m][n] = MFMA16(wf[n], of[m], acc[m][n]);
  }
#pragma unroll
  for (int m = 0; m < 4; ++m)
#pragma unroll
    for (int n = 0; n < 5; ++n) *(f32x4*)(lds + wid * O_WAVE + (m * 16 + c16) * O_ROW + (n * 16 + g * 4) * 4) = acc[m][n];
  __syncthreads();
  for (int task = tid; task < 640; task += 256) {
    const int row = task / 10, oc = task - row * 10;
    const char* p = lds + row * O_ROW + oc * 32;
    f32x4 a = *(const f32x4*)p, b = *(const f32x4*)(p + 16);
#pragma unroll
    for (int w = 1; w < 4; ++w) { a += *(const f32x4*)(p + w * O_WAVE); b += *(const f32x4*)(p + w * O_WAVE + 16); }
    u32x4 v = {cvtpk(a[0], a[1]), cvtpk(a[2], a[3]), cvtpk(b[0], b[1]), cvtpk(b[2], b[3])};
    *(u32x4*)(out + (size_t)(mt * 64 + row) * D + nb * 80 + oc * 8) = v;
  }
}

extern "C" void kernel_launch(void* const* d_in, const int* in_sizes, int n_in, void* d_out, int out_size, void* d_ws, size_t ws_size,
                              hipStream_t stream) {
  const u16* x = (const u16*)d_in[0];
  const u16* wqkv = (const u16*)d_in[1];
  const u16* woa = (const u16*)d_in[2];
  const u16* wq = (const u16*)d_in[4];
  const u16* wk = (const u16*)d_in[5];
  const u16* wv = (const u16*)d_in[6];
  const u16* wob = (const u16*)d_in[7];
  const u16* gw = (const u16*)d_in[8];
  char* ws = (char*)d_ws;
  k1_proj<<<256, 512, 0, stream>>>(x, wqkv, wq, wk, wv, gw, ws);
  k2_attn<<<256, 512, 0, stream>>>(woa, wob, ws);
  k3_out<<<256, 256, 0, stream>>>(ws, (u16*)d_out);
}
```

```cpp
#include <hip/hip_runtime.h>
#include <hip/hip_bf16.h>
#include <stdint.h>
#include <type_traits>

typedef __attribute__((ext_vector_type(8))) short bf16x8;
typedef __attribute__((ext_vector_type(4))) short bf16x4;
typedef __attribute__((ext_vector_type(16))) float f32x16;
typedef __attribute__((ext_vector_type(4))) float f32x4;
typedef __attribute__((ext_vector_type(2))) float f32x2;
typedef __attribute__((ext_vector_type(4))) unsigned u32x4;
typedef __attribute__((ext_vector_type(2))) unsigned u32x2;
typedef __attribute__((ext_vector_type(2))) __bf16 bf16x2_t;
typedef __attribute__((address_space(3))) const char* lds_cptr;
typedef unsigned short u16;

#define DI __device__ __forceinline__
#define SBAR() __builtin_amdgcn_sched_barrier(0)
#define PIN(x) asm volatile("" : "+v"(x))
#define MFMA32(a, b, c) __builtin_amdgcn_mfma_f32_32x32x16_bf16((a), (b), (c), 0, 0, 0)
#define MFMA8(a, b, c) __builtin_amdgcn_mfma_f32_32x32x8bf16_1k((a), (b), (c), 0, 0, 0)
#define MFMA16(a, b, c) __builtin_amdgcn_mfma_f32_16x16x32_bf16((a), (b), (c), 0, 0, 0)

#define STAMP_DECL
#define STAMP(i)
#define STAMP_OUT(kid, wsb)

constexpr int S = 2048, D = 640;
constexpr float QSCALE = 0.22360679774997896f * 1.4426950408889634f;

constexpr size_t HEAD_B = (size_t)S * 48;
constexpr size_t WS_Q = 0;
constexpr size_t WS_K = WS_Q + 64 * HEAD_B;
constexpr size_t WS_V = WS_K + 33 * HEAD_B;
constexpr size_t WS_G = WS_V + 33 * HEAD_B;
constexpr size_t WS_O = WS_G + S * 4;
constexpr size_t WS_W = WS_O + (size_t)160 * S * 16;
constexpr size_t WS_END = WS_W + (size_t)160 * D * 16;

DI unsigned cvtpk(float lo, float hi) {
  f32x2 v = {lo, hi};
  return __builtin_bit_cast(unsigned, __builtin_convertvector(v, bf16x2_t));
}
DI void glds16(const void* sbase, unsigned voff, unsigned lds_dst) {
  unsigned keep;
  asm volatile("s_mov_b32 %0, m0\n\ts_mov_b32 m0, %3\n\ts_nop 0\n\tglobal_load_lds_dwordx4 %1, %2\n\ts_mov_b32 m0, %0"
               : "=&s"(keep) : "v"(voff), "s"(sbase), "s"(lds_dst) : "memory");
}
DI bf16x8 lds_b128(lds_cptr p) { return *(const __attribute__((address_space(3))) bf16x8*)p; }
DI bf16x4 lds_b64(lds_cptr p) { return *(const __attribute__((address_space(3))) bf16x4*)p; }
DI bf16x4 lds_tr(lds_cptr p) {
  return __builtin_amdgcn_ds_read_tr16_b64_v4i16((__attribute__((address_space(3))) bf16x4*)p);
}
DI bf16x8 cat4(bf16x4 a, bf16x4 b) { return __builtin_shufflevector(a, b, 0, 1, 2, 3, 4, 5, 6, 7); }
#define WAIT_BAR(N) asm volatile("s_waitcnt vmcnt(" #N ") lgkmcnt(0)\n\ts_barrier" ::: "memory")

constexpr int P_XB = 16384, P_WB = 20480, P_EB = 3072, P_STAGE = P_XB + P_WB + P_EB, P_NST = 4;
constexpr int P_GATE = P_STAGE * P_NST, P_LDS = P_GATE + 2048;

__global__ __launch_bounds__(512, 1) void k1_proj(const u16* __restrict__ x, const u16* __restrict__ wqkv, const u16* __restrict__ wq,
                                                  const u16* __restrict__ wk, const u16* __restrict__ wv, const u16* __restrict__ gw,
                                                  char* __restrict__ ws) {
  __shared__ __attribute__((aligned(16))) char lds[P_LDS];
  STAMP_DECL STAMP(0);
  const int tid = threadIdx.x, lane = tid & 63, r = lane & 31, h = lane >> 5;
  const int wid = __builtin_amdgcn_readfirstlane(tid >> 6);
  const int xcd = blockIdx.x & 7, bi = blockIdx.x >> 3;
  const int mt = (xcd & 1) * 8 + (bi & 7), j = (xcd >> 1) * 4 + (bi >> 3);
  const int m0 = mt * 128;
  const int typ = j >> 2;
  const bool wg_extra = typ == 0 || typ == 3;
  const int ex_rt = j & 3, ex_typ = typ == 3;
  const unsigned lds0 = (unsigned)(uintptr_t)lds;
  const lds_cptr L = (lds_cptr)lds;
  const bool vtype = typ == 2;
  const int rp = wid & 1, grp = wid >> 1;
  const bool my_extra = wg_extra && grp == 1 && rp == (ex_rt >> 1);
  const int rta = 2 * rp + (ex_rt & 1), rtb = 2 * rp + 1 - (ex_rt & 1);
  f32x16 acc[2][3] = {};

  if (wid >= 4) {
    const int lw = wid - 4;
    unsigned xoff[2], woff[5];
#pragma unroll
    for (int i = 0; i < 2; ++i) {
      const int row = lw * 32 + i * 8 + (lane >> 3), c = (lane & 7) ^ ((row >> 1) & 7);
      xoff[i] = (unsigned)(m0 + row) * 1280u + c * 16;
    }
    const u16* wsrc = (typ < 3) ? (wqkv + j * 160) : (wq + (j - 12) * 160);
    const unsigned ldw = (typ < 3) ? 3840u : 1280u;
    const u16* esrc = ex_typ ? wv : wk;
#pragma unroll
    for (int i = 0; i < 5; ++i) {
      const int idx = i * 64 + lane, row = (idx * 205) >> 12, gr = idx - row * 20;
      woff[i] = (unsigned)(lw * 16 + row) * ldw + gr * 16;
    }
    auto stage = [&](int s) {
      const unsigned dst = lds0 + (s & 3) * P_STAGE;
      const char* xb = (const char*)x + s * 128;
      const char* wb = (const char*)wsrc + (size_t)s * 64 * ldw;
#pragma unroll
      for (int i = 0; i < 4; ++i) glds16(xb + (i >> 1) * 16 * 1280, xoff[i & 1], dst + (lw * 4 + i) * 1024);
#pragma unroll
      for (int i = 0; i < 5; ++i) glds16(wb, woff[i], dst + P_XB + (lw * 5 + i) * 1024);
      if (wg_extra && lw < 3) {
        if (lw < 2 || lane < 32) glds16((const char*)esrc + s * 2560 + lw * 1024, lane * 16, dst + P_XB + P_WB + lw * 1024);
      }
    };
    if (typ == 0 && lw == 3) {
      glds16(gw, lane * 16, lds0 + P_GATE);
      if (lane < 16) glds16((const char*)gw + 1024, lane * 16, lds0 + P_GATE + 1024);
    }
    stage(0); stage(1);
    WAIT_BAR(9);
    stage(2);
    WAIT_BAR(9);
    stage(3);
#pragma unroll 1
    for (int s = 2; s < 10; ++s) {
      if (s + 2 < 10) stage(s + 2);
      if (s <= 7) WAIT_BAR(18); else if (s == 8) WAIT_BAR(9); else WAIT_BAR(0);
    }
  } else {
    const int xsw = h ^ ((r >> 1) & 7);
    const int xa = (rta * 32 + r) * 128, xb = (rtb * 32 + r) * 128;
    const int q4 = (lane & 15) >> 2, p4 = lane & 3, b16 = (lane >> 4) & 1;
    const int wbase = P_XB + (8 * h + q4) * 320 + (16 * b16 + 4 * p4) * 2 + grp * 192;
    const int ebase = P_XB + P_WB + (8 * h + q4) * 40 + (16 * b16 + 4 * p4) * 2;
    auto mainloop = [&](auto vt_, auto mode_) {
      constexpr bool VT = decltype(vt_)::value;
      constexpr int MODE = decltype(mode_)::value;
      struct Frag { bf16x8 xa, xb, w0, w1, w2, g; };
      auto rd = [&](Frag& f, int s, int ks) {
        const lds_cptr st = L + (s & 3) * P_STAGE;
        const lds_cptr wp = st + wbase + ks * 16 * 320;
        f.xa = lds_b128(st + xa + ((2 * ks) ^ xsw) * 16);
        f.w0 = cat4(lds_tr(wp), lds_tr(wp + 4 * 320));
        f.xb = lds_b128(st + xb + ((2 * ks) ^ xsw) * 16);
        f.w1 = cat4(lds_tr(wp + 64), lds_tr(wp + 64 + 4 * 320));
        if constexpr (MODE == 0) f.w2 = cat4(lds_tr(wp + 128), lds_tr(wp + 128 + 4 * 320));
        if constexpr (MODE >= 2) {
          const lds_cptr ep = st + ebase + ks * 16 * 40;
          f.w2 = cat4(lds_tr(ep), lds_tr(ep + 4 * 40));
          if constexpr (MODE == 2) f.g = lds_b128(L + P_GATE + (s * 64 + ks * 16 + 8 * h) * 2);
        }
      };
      auto mm = [&](f32x16& a, const bf16x8& xf, const bf16x8& wf, auto v_) {
        if constexpr (decltype(v_)::value) a = MFMA32(xf, wf, a); else a = MFMA32(wf, xf, a);
      };
      auto mmA = [&](const Frag& f) {
        mm(acc[0][0], f.xa, f.w0, vt_); mm(acc[1][0], f.xb, f.w0, vt_); mm(acc[0][1], f.xa, f.w1, vt_);
        if constexpr (MODE == 0) { mm(acc[0][2], f.xa, f.w2, vt_); mm(acc[1][2], f.xb, f.w2, vt_); }
        if constexpr (MODE == 2) acc[0][2] = MFMA32(r == 20 ? f.g : f.w2, f.xa, acc[0][2]);
        if constexpr (MODE == 3) acc[0][2] = MFMA32(f.xa, f.w2, acc[0][2]);
      };
      auto mmB = [&](const Frag& f) { mm(acc[1][1], f.xb, f.w1, vt_); };
      auto ilv = [&]() {
#pragma unroll
        for (int i = 0; i < (MODE == 0 ? 6 : (MODE == 1 ? 4 : 5)); ++i) {
          __builtin_amdgcn_sched_group_barrier(0x008, 1, 0);
          __builtin_amdgcn_sched_group_barrier(0x100, 2, 0);
        }
      };
      Frag f0, f1;
      asm volatile("s_barrier" ::: "memory");
      rd(f0, 0, 0);
      SBAR();
#pragma unroll 1
      for (int s = 0; s < 9; ++s) {
        rd(f1, s, 1); mmA(f0); mmB(f0); ilv(); SBAR();
        rd(f0, s, 2); mmA(f1); mmB(f1); ilv(); SBAR();
        rd(f1, s, 3); mmA(f0); mmB(f0); ilv(); SBAR();
        mmA(f1); SBAR();
        asm volatile("s_barrier" ::: "memory");
        mmB(f1); SBAR();
        rd(f0, s + 1, 0); SBAR();
      }
      rd(f1, 9, 1); mmA(f0); mmB(f0); ilv(); SBAR();
      rd(f0, 9, 2); mmA(f1); mmB(f1); ilv(); SBAR();
      rd(f1, 9, 3); mmA(f0); mmB(f0); ilv(); SBAR();
      mmA(f1); mmB(f1);
    };
    using std::integral_constant;
    if (vtype) {
      if (grp == 0) mainloop(integral_constant<bool, true>{}, integral_constant<int, 0>{});
      else          mainloop(integral_constant<bool, true>{}, integral_constant<int, 1>{});
    } else if (grp == 0) mainloop(integral_constant<bool, false>{}, integral_constant<int, 0>{});
    else if (!my_extra)  mainloop(integral_constant<bool, false>{}, integral_constant<int, 1>{});
    else if (ex_typ == 0) mainloop(integral_constant<bool, false>{}, integral_constant<int, 2>{});
    else                  mainloop(integral_constant<bool, false>{}, integral_constant<int, 3>{});
  }
  STAMP(2);

  const int ntile = grp == 0 ? 3 : 2;
  if (!vtype) {
    char* base = ws + (typ == 1 ? WS_K + (size_t)(j - 4) * 8 * HEAD_B : WS_Q + (size_t)(typ == 0 ? j : j - 8) * 8 * HEAD_B) + (size_t)m0 * 48 + r * 8;
    if (wid < 4) {
      const f32x2 sc = {typ == 1 ? 1.0f : QSCALE, typ == 1 ? 1.0f : QSCALE};
#pragma unroll
      for (int t = 0; t < 3; ++t) {
        if (t < ntile) {
#pragma unroll
          for (int g = 0; g < 4; ++g) {
            const int qd = 8 * (grp * 3 + t) + 2 * g + h, hl = (qd * 205) >> 10, dq = qd - hl * 5;
            const unsigned off = (unsigned)hl * (unsigned)HEAD_B + dq * 256;
#pragma unroll
            for (int i = 0; i < 2; ++i) {
              const f32x16& a = acc[i][t];
              const f32x2 lo = (f32x2){a[4 * g], a[4 * g + 1]} * sc, hi = (f32x2){a[4 * g + 2], a[4 * g + 3]} * sc;
              *(u32x2*)(base + (i ? rtb : rta) * 1536 + off) = (u32x2){cvtpk(lo[0], lo[1]), cvtpk(hi[0], hi[1])};
            }
          }
        }
      }
    } else if (typ == 1) {
#pragma unroll
      for (int i = 0; i < 4; ++i) *(u32x2*)(base + (wid - 4) * 1536 + (size_t)(2 * i + h) * HEAD_B + 5 * 256) = (u32x2){0x3F80u, 0u};
    }
  } else {
    char* base = ws + WS_V + (size_t)(j - 8) * 8 * HEAD_B + (size_t)(m0 / 16) * 768 + h * 384;
    if (wid < 4) {
#pragma unroll
      for (int t = 0; t < 3; ++t) {
        if (t < ntile) {
          const int n = 32 * (grp * 3 + t) + r, hl = (n * 205) >> 12, d = n - hl * 20;
#pragma unroll
          for (int i = 0; i < 2; ++i) {
            const f32x16& a = acc[i][t];
#pragma unroll
            for (int sp = 0; sp < 2; ++sp) {
              u32x4 v = {cvtpk(a[8 * sp], a[8 * sp + 1]), cvtpk(a[8 * sp + 2], a[8 * sp + 3]),
                         cvtpk(a[8 * sp + 4], a[8 * sp + 5]), cvtpk(a[8 * sp + 6], a[8 * sp + 7])};
              *(u32x4*)(base + (size_t)hl * HEAD_B + (i ? rtb : rta) * 1536 + sp * 768 + d * 16) = v;
            }
          }
        }
      }
    } else {
      const unsigned cv = (r & 3) == 0 ? 0x3F803F80u : 0u;
#pragma unroll
      for (int sp = 0; sp < 2; ++sp)
        *(u32x4*)(base + (size_t)(r >> 2) * HEAD_B + (wid - 4) * 1536 + sp * 768 + (20 + (r & 3)) * 16) = (u32x4){cv, cv, cv, cv};
    }
  }
  if (my_extra) {
    const f32x16& e = acc[0][2];
    const int tok = m0 + rta * 32 + r;
    if (ex_typ == 0) {
      char* base = ws + WS_K + (size_t)32 * HEAD_B + (size_t)(m0 + rta * 32) * 48 + r * 8;
#pragma unroll
      for (int g = 0; g < 3; ++g) {
        u32x2 v = {cvtpk(e[4 * g], e[4 * g + 1]), cvtpk(e[4 * g + 2], e[4 * g + 3])};
        if (g == 2 && h == 1) v = (u32x2){0x3F80u, 0u};
        *(u32x2*)(base + (2 * g + h) * 256) = v;
      }
      if (h == 1) *(float*)(ws + WS_G + (size_t)tok * 4) = 1.0f / (1.0f + __expf(-e[8]));
    } else if (r < 24) {
      char* base = ws + WS_V + (size_t)32 * HEAD_B + (size_t)((m0 + rta * 32) / 16) * 768 + h * 384 + r * 16;
      const unsigned cv = r == 20 ? 0x3F803F80u : 0u;
#pragma unroll
      for (int sp = 0; sp < 2; ++sp) {
        u32x4 v = {cvtpk(e[8 * sp], e[8 * sp + 1]), cvtpk(e[8 * sp + 2], e[8 * sp + 3]),
                   cvtpk(e[8 * sp + 4], e[8 * sp + 5]), cvtpk(e[8 * sp + 6], e[8 * sp + 7])};
        if (r >= 20) v = (u32x4){cv, cv, cv, cv};
        *(u32x4*)(base + sp * 768) = v;
      }
    }
  }
  STAMP(3);
  STAMP_OUT(0, ws);
}

constexpr int A_CHUNK = 24576, A_KB = 12288, A_NSLOT = 3, A_LDS = A_CHUNK * A_NSLOT + 1024;

DI float half_max(float m) {
  auto rr = __builtin_amdgcn_permlane32_swap(__float_as_uint(m), __float_as_uint(m), false, false);
  return fmaxf(__uint_as_float(rr[0]), __uint_as_float(rr[1]));
}
DI float max16(const f32x16& s) {
  float a = fmaxf(fmaxf(s[0], s[1]), s[2]), b = fmaxf(fmaxf(s[3], s[4]), s[5]);
  a = fmaxf(fmaxf(a, s[6]), s[7]); b = fmaxf(fmaxf(b, s[8]), s[9]);
  a = fmaxf(fmaxf(a, s[10]), s[11]); b = fmaxf(fmaxf(b, s[12]), s[13]);
  a = fmaxf(fmaxf(a, s[14]), s[15]);
  return half_max(fmaxf(a, b));
}
DI f32x16 attn_safe(const char* kg, const char* vg, bf16x8 q1, bf16x4 q2, int r, int h) {
  if (h) q2 = (bf16x4){0, 0, 0, 0};
  f32x16 o = {};
  float m = -INFINITY;
#pragma unroll 1
  for (int t = 0; t < 64; ++t) {
    const char* kp = kg + t * 1536 + r * 8;
    const bf16x8 k1 = cat4(*(const bf16x4*)(kp + h * 512), *(const bf16x4*)(kp + h * 512 + 256));
    const bf16x4 k2 = *(const bf16x4*)(kp + 1024 + h * 256);
    const int vr = r < 24 ? r : 23;
    const bf16x8 v0 = *(const bf16x8*)(vg + t * 1536 + h * 384 + vr * 16);
    const bf16x8 v1 = *(const bf16x8*)(vg + t * 1536 + 768 + h * 384 + vr * 16);
    f32x16 s = {};
    s = MFMA32(k1, q1, s);
    s = MFMA8(k2, q2, s);
    const float mn = fmaxf(m, max16(s));
    const float al = __builtin_amdgcn_exp2f(m - mn);
    m = mn;
#pragma unroll
    for (int i = 0; i < 16; ++i) { o[i] *= al; s[i] = __builtin_amdgcn_exp2f(s[i] - mn); }
    u32x4 p0 = {cvtpk(s[0], s[1]), cvtpk(s[2], s[3]), cvtpk(s[4], s[5]), cvtpk(s[6], s[7])};
    u32x4 p1 = {cvtpk(s[8], s[9]), cvtpk(s[10], s[11]), cvtpk(s[12], s[13]), cvtpk(s[14], s[15])};
    o = MFMA32(v0, __builtin_bit_cast(bf16x8, p0), o);
    o = MFMA32(v1, __builtin_bit_cast(bf16x8, p1), o);
  }
  return o;
}
DI bool o_unsafe(const f32x16& o, int h) {
  float a = fabsf(o[0]) + fabsf(o[1]), b = fabsf(o[2]) + fabsf(o[3]);
#pragma unroll
  for (int i = 4; i < 8; i += 2) { a += fabsf(o[i]); b += fabsf(o[i + 1]); }
  float c = fabsf(o[8]) + fabsf(o[9]) + fabsf(o[10]) + fabsf(o[11]);
  if (h) c = (o[8] < 1.0e18f && o[8] > 1.0e-18f) ? 0.f : INFINITY;
  return !(a + b + c < 3.0e38f);
}
DI void attn_store(const f32x16& o, char* og, int hh, int tok, float gfac, int h) {
  auto rr = __builtin_amdgcn_permlane32_swap(__float_as_uint(o[8]), __float_as_uint(o[8]), false, false);
  const float sc = gfac * __builtin_amdgcn_rcpf(__uint_as_float(rr[1]));
  u32x4 v = {cvtpk(o[0] * sc, o[1] * sc), cvtpk(o[2] * sc, o[3] * sc), cvtpk(o[4] * sc, o[5] * sc), cvtpk(o[6] * sc, o[7] * sc)};
  *(u32x4*)(og + ((size_t)(hh * 2 + h) * S + tok) * 16) = v;
  if (h == 0) {
    u32x2 t = {cvtpk(o[8] * sc, o[9] * sc), cvtpk(o[10] * sc, o[11] * sc)};
    *(u32x2*)(og + ((size_t)(128 + (hh >> 1)) * S + tok) * 16 + (hh & 1) * 8) = t;
  }
}

__global__ __launch_bounds__(512, 1) void k2_attn(const u16* __restrict__ woa, const u16* __restrict__ wob, char* __restrict__ ws) {
  __shared__ __attribute__((aligned(16))) char lds[A_LDS];
  STAMP_DECL STAMP(0);
  const int tid = threadIdx.x, lane = tid & 63, r = lane & 31, h = lane >> 5;
  const int wid = __builtin_amdgcn_readfirstlane(tid >> 6);
  const int xcd = blockIdx.x & 7, bi = blockIdx.x >> 3;
  const int hh = (bi >> 4) * 32 + xcd * 4 + ((bi >> 2) & 3), qb = bi & 3;
  const int kvh = hh < 32 ? hh : 32;
  const char* kg = ws + WS_K + (size_t)kvh * HEAD_B;
  const char* vg = ws + WS_V + (size_t)kvh * HEAD_B;
  const int tokA = qb * 512 + wid * 64 + r, tokB = tokA + 32;
  const unsigned lds0 = (unsigned)(uintptr_t)lds;
  const lds_cptr L = (lds_cptr)lds;

  const char* qg = ws + WS_Q + (size_t)hh * HEAD_B;
  const char* qp = qg + (tokA - r) * 48 + r * 8;
  const bf16x8 qA1 = cat4(*(const bf16x4*)(qp + h * 512), *(const bf16x4*)(qp + h * 512 + 256));
  const bf16x8 qB1 = cat4(*(const bf16x4*)(qp + 1536 + h * 512), *(const bf16x4*)(qp + 1536 + h * 512 + 256));
  bf16x4 qA2 = *(const bf16x4*)(qp + 1024), qB2 = *(const bf16x4*)(qp + 1536 + 1024);

  const float gA = *(const float*)(ws + WS_G + tokA * 4), gB = *(const float*)(ws + WS_G + tokB * 4);

  auto dma = [&](int c, int slot) {
#pragma unroll
    for (int i = 0; i < 3; ++i) {
      const int pc = wid * 3 + i;
      const char* src = pc < 12 ? kg + c * A_KB + pc * 1024 : vg + c * A_KB + (pc - 12) * 1024;
      glds16(src, lane * 16, lds0 + slot * A_CHUNK + pc * 1024);
    }
  };
  dma(0, 0);
  const int task = blockIdx.x * 400 + tid;
  if (tid < 400) {
    u16 wv8[8];
    const int gr = task / 640, n = task - gr * 640;
    const bool mainp = gr < 128;
    const int hd0 = mainp ? (gr >> 1) : 2 * (gr - 128);
    const u16* w = (hd0 < 32 ? woa + hd0 * 20 * 640 : wob + (hd0 - 32) * 20 * 640) + (mainp ? 4 * (gr & 1) : 16) * 640 + n;
    const int q2 = mainp ? 8 * 640 : 20 * 640;
#pragma unroll
    for (int jj = 0; jj < 4; ++jj) { wv8[jj] = w[jj * 640]; wv8[4 + jj] = w[q2 + jj * 640]; }
    u32x4 v = {wv8[0] | ((unsigned)wv8[1] << 16), wv8[2] | ((unsigned)wv8[3] << 16), wv8[4] | ((unsigned)wv8[5] << 16), wv8[6] | ((unsigned)wv8[7] << 16)};
    *(u32x4*)(ws + WS_W + (size_t)task * 16) = v;
  }
  if (wid == 7) asm volatile("s_waitcnt vmcnt(0)" ::: "memory");
  WAIT_BAR(1);
  STAMP(1);
  dma(1, 1);

  const int kb1 = r * 8 + h * 512, kb2 = r * 8 + 1024 + h * 256, vb = A_KB + h * 384 + (r < 21 ? r : 21) * 16;
  int kb1b = kb1 + 256; PIN(kb1b);
  bf16x8 k1a = cat4(lds_b64(L + kb1), lds_b64(L + kb1b)), k1b;
  bf16x4 k2a = lds_b64(L + kb2), k2b;
  bf16x8 v0a = {}, v1a = {}, v0b = {}, v1b = {};
  const bf16x4 q2keepA = qA2, q2keepB = qB2;
  f32x16 sA, sB, oA = {}, oB = {};
  const f32x16 zero = {};
  {
    if (h) { qA2 = (bf16x4){0, 0, 0, 0}; qB2 = (bf16x4){0, 0, 0, 0}; }
    sA = MFMA32(k1a, qA1, zero); sB = MFMA32(k1a, qB1, zero);
    sA = MFMA8(k2a, qA2, sA);    sB = MFMA8(k2a, qB2, sB);
    const unsigned ma = cvtpk(-max16(sA), 0.f), mb = cvtpk(-max16(sB), 0.f);
    if (h) { qA2 = __builtin_bit_cast(bf16x4, (u32x2){ma, 0u}); qB2 = __builtin_bit_cast(bf16x4, (u32x2){mb, 0u}); }
    sA = MFMA32(k1a, qA1, zero);
    sA = MFMA8(k2a, qA2, sA);
  }
  u32x4 pA0 = {}, pA1 = {}, pB0 = {}, pB1 = {};

#define PRIO(n) __builtin_amdgcn_s_setprio(n)
#define EX(v) __builtin_amdgcn_exp2f(v)
#define GAP(MF, X, i, P, e) do { MF; SBAR(); X[i] = EX(X[i]); X[i + 1] = EX(X[i + 1]); X[i + 2] = EX(X[i + 2]); X[i + 3] = EX(X[i + 3]); \
    P[e] = cvtpk(X[i], X[i + 1]); P[e + 1] = cvtpk(X[i + 2], X[i + 3]); PIN(P); SBAR(); } while (0)
#define BF(p) __builtin_bit_cast(bf16x8, p)
#define PHASE(X, PX0, PX1, Y, OY, PY0, PY1, QY1, QY2, K1, K2, V0, V1) do { \
    GAP(Y = MFMA32(K1, QY1, zero), X, 0, PX0, 0);  \
    GAP(Y = MFMA8(K2, QY2, Y),     X, 4, PX0, 2);  \
    GAP(OY = MFMA32(V0, BF(PY0), OY), X, 8, PX1, 0);  \
    GAP(OY = MFMA32(V1, BF(PY1), OY), X, 12, PX1, 2); } while (0)
#define TILE(KP, VP, K1C, K2C, V0C, V1C, K1N, K2N, V0N, V1N) do { \
    K1N = cat4(lds_b64((KP) + kb1), lds_b64((KP) + kb1b)); K2N = lds_b64((KP) + kb2); V0N = lds_b128((VP) + vb); V1N = lds_b128((VP) + vb + 768); SBAR(); \
    PHASE(sA, pA0, pA1, sB, oB, pB0, pB1, qB1, qB2, K1C, K2C, V0C, V1C); \
    __builtin_amdgcn_s_waitcnt(0xC07F); SBAR(); \
    PHASE(sB, pB0, pB1, sA, oA, pA0, pA1, qA1, qA2, K1N, K2N, V0N, V1N); } while (0)

  STAMP(2);
  int slot = 0;
#pragma unroll 1
  for (int c = 0; c < 8; ++c) {
    const int nslot = slot == A_NSLOT - 1 ? 0 : slot + 1;
    const lds_cptr cp = L + slot * A_CHUNK, np = L + nslot * A_CHUNK;
    PRIO(3);
    TILE(cp + 1 * 1536, cp + 0 * 1536, k1a, k2a, v0a, v1a, k1b, k2b, v0b, v1b);
    TILE(cp + 2 * 1536, cp + 1 * 1536, k1b, k2b, v0b, v1b, k1a, k2a, v0a, v1a);
    PRIO(2);
    TILE(cp + 3 * 1536, cp + 2 * 1536, k1a, k2a, v0a, v1a, k1b, k2b, v0b, v1b);
    TILE(cp + 4 * 1536, cp + 3 * 1536, k1b, k2b, v0b, v1b, k1a, k2a, v0a, v1a);
    PRIO(1);
    TILE(cp + 5 * 1536, cp + 4 * 1536, k1a, k2a, v0a, v1a, k1b, k2b, v0b, v1b);
    TILE(cp + 6 * 1536, cp + 5 * 1536, k1b, k2b, v0b, v1b, k1a, k2a, v0a, v1a);
    PRIO(0);
    TILE(cp + 7 * 1536, cp + 6 * 1536, k1a, k2a, v0a, v1a, k1b, k2b, v0b, v1b);
    WAIT_BAR(0);
    if (c + 2 < 8) dma(c + 2, nslot == A_NSLOT - 1 ? 0 : nslot + 1);
    PRIO(3);
    TILE(np, cp + 7 * 1536, k1b, k2b, v0b, v1b, k1a, k2a, v0a, v1a);
    slot = nslot;
  }
  STAMP(3);
  oB = MFMA32(v0a, BF(pB0), oB);
  oB = MFMA32(v1a, BF(pB1), oB);

  if (__builtin_expect(__any(o_unsafe(oA, h) || o_unsafe(oB, h)), 0)) {
    oA = attn_safe(kg, vg, qA1, q2keepA, r, h);
    oB = attn_safe(kg, vg, qB1, q2keepB, r, h);
  }
  char* og = ws + WS_O;
  attn_store(oA, og, hh, tokA, hh < 32 ? gA : 1.0f - gA, h);
  attn_store(oB, og, hh, tokB, hh < 32 ? gB : 1.0f - gB, h);
  STAMP(4);
  STAMP_OUT(1, ws);
}

constexpr int O_LDS = 4 * 4 * 5 * 1024;

__global__ __launch_bounds__(256, 1) void k3_out(const char* __restrict__ ws, u16* __restrict__ out) {
  __shared__ __attribute__((aligned(16))) char lds[O_LDS];
  STAMP_DECL STAMP(0);
  const int tid = threadIdx.x, lane = tid & 63, c16 = lane & 15, g = lane >> 4;
  const int wid = __builtin_amdgcn_readfirstlane(tid >> 6);
  const int xcd = blockIdx.x & 7, bi = blockIdx.x >> 3;
  const int mt = (xcd >> 1) * 8 + (bi & 7), nb = (xcd & 1) * 4 + (bi >> 3);
  const char* og = ws + WS_O + ((size_t)(wid * 40 + g) * S + mt * 64 + c16) * 16;
  const char* wg = ws + WS_W + ((size_t)(wid * 40 + g) * D + nb * 80 + c16) * 16;
  int mo[4];
#pragma unroll
  for (int m = 0; m < 4; ++m) mo[m] = ((m + wid) & 3) * 256;
  f32x4 acc[4][5] = {};
  bf16x8 of[10][4], wf[10][5];
  auto load = [&](int s) {
#pragma unroll
    for (int n = 0; n < 5; ++n) wf[s][n] = *(const bf16x8*)(wg + (size_t)s * 4 * D * 16 + n * 256);
#pragma unroll
    for (int m = 0; m < 4; ++m) of[s][m] = *(const bf16x8*)(og + (size_t)s * 4 * S * 16 + mo[m]);
  };
#pragma unroll
  for (int s = 0; s < 6; ++s) load(s);
  SBAR();
#pragma unroll
  for (int s = 0; s < 10; ++s) {
#pragma unroll
    for (int m = 3; m >= 0; --m)
#pragma unroll
      for (int n = 0; n < 5; ++n) acc[m][n] = MFMA16(wf[s][n], of[s][m], acc[m][n]);
    SBAR();
    if (s + 6 < 10) load(s + 6);
    SBAR();
  }
  STAMP(1);
#pragma unroll
  for (int m = 1; m < 4; ++m)
#pragma unroll
    for (int n = 0; n < 5; ++n) *(f32x4*)(lds + ((((m + wid) & 3) * 4 + m) * 5 + n) * 1024 + lane * 16) = acc[m][n];
  __syncthreads();
  u16* op = out + (size_t)(mt * 64 + wid * 16 + c16) * D + nb * 80 + g * 4;
#pragma unroll
  for (int n = 0; n < 5; ++n) {
    f32x4 a = acc[0][n];
#pragma unroll
    for (int m = 1; m < 4; ++m) a += *(const f32x4*)(lds + ((wid * 4 + m) * 5 + n) * 1024 + lane * 16);
    *(u32x2*)(op + n * 16) = (u32x2){cvtpk(a[0], a[1]), cvtpk(a[2], a[3])};
  }
  STAMP(2);
  STAMP_OUT(2, (char*)ws);
}

extern "C" void kernel_launch(void* const* d_in, const int* in_sizes, int n_in, void* d_out, int out_size, void* d_ws, size_t ws_size,
                              hipStream_t stream) {
  const u16* x = (const u16*)d_in[0];
  const u16* wqkv = (const u16*)d_in[1];
  const u16* woa = (const u16*)d_in[2];
  const u16* wq = (const u16*)d_in[4];
  const u16* wk = (const u16*)d_in[5];
  const u16* wv = (const u16*)d_in[6];
  const u16* wob = (const u16*)d_in[7];
  const u16* gw = (const u16*)d_in[8];
  char* ws = (char*)d_ws;
  k1_proj<<<256, 512, 0, stream>>>(x, wqkv, wq, wk, wv, gw, ws);
  k2_attn<<<256, 512, 0, stream>>>(woa, wob, ws);
  k3_out<<<256, 256, 0, stream>>>(ws, (u16*)d_out);
}
```

```cpp
#include <hip/hip_runtime.h>
#include <hip/hip_bf16.h>
#include <stdint.h>
#include <type_traits>

typedef __attribute__((ext_vector_type(8))) short bf16x8;
typedef __attribute__((ext_vector_type(4))) short bf16x4;
typedef __attribute__((ext_vector_type(16))) float f32x16;
typedef __attribute__((ext_vector_type(4))) float f32x4;
typedef __attribute__((ext_vector_type(2))) float f32x2;
typedef __attribute__((ext_vector_type(4))) unsigned u32x4;
typedef __attribute__((ext_vector_type(2))) unsigned u32x2;
typedef __attribute__((ext_vector_type(2))) __bf16 bf16x2_t;
typedef __attribute__((address_space(3))) const char* lds_cptr;
typedef unsigned short u16;

#define DI __device__ __forceinline__
#define SBAR() __builtin_amdgcn_sched_barrier(0)
#define PIN(x) asm volatile("" : "+v"(x))
#define MFMA32(a, b, c) __builtin_amdgcn_mfma_f32_32x32x16_bf16((a), (b), (c), 0, 0, 0)
#define MFMA8(a, b, c) __builtin_amdgcn_mfma_f32_32x32x8bf16_1k((a), (b), (c), 0, 0, 0)
#define MFMA16(a, b, c) __builtin_amdgcn_mfma_f32_16x16x32_bf16((a), (b), (c), 0, 0, 0)

#define STAMP_DECL
#define STAMP(i)
#define STAMP_OUT(kid, wsb)

constexpr int S = 2048, D = 640;
constexpr float QSCALE = 0.22360679774997896f * 1.4426950408889634f;

constexpr size_t HEAD_B = (size_t)S * 48;
constexpr size_t WS_Q = 0;
constexpr size_t WS_K = WS_Q + 64 * HEAD_B;
constexpr size_t WS_V = WS_K + 33 * HEAD_B;
constexpr size_t WS_G = WS_V + 33 * HEAD_B;
constexpr size_t WS_O = WS_G + S * 4;
constexpr size_t WS_W = WS_O + (size_t)160 * S * 16;
constexpr size_t WS_F = WS_W + (size_t)160 * D * 16;
constexpr size_t WS_END = WS_F + 8192;

DI unsigned cvtpk(float lo, float hi) {
  f32x2 v = {lo, hi};
  return __builtin_bit_cast(unsigned, __builtin_convertvector(v, bf16x2_t));
}
DI void glds16(const void* sbase, unsigned voff, unsigned lds_dst) {
  unsigned keep;
  asm volatile("s_mov_b32 %0, m0\n\ts_mov_b32 m0, %3\n\ts_nop 0\n\tglobal_load_lds_dwordx4 %1, %2\n\ts_mov_b32 m0, %0"
               : "=&s"(keep) : "v"(voff), "s"(sbase), "s"(lds_dst) : "memory");
}
DI bf16x8 lds_b128(lds_cptr p) { return *(const __attribute__((address_space(3))) bf16x8*)p; }
DI bf16x4 lds_b64(lds_cptr p) { return *(const __attribute__((address_space(3))) bf16x4*)p; }
DI bf16x4 lds_tr(lds_cptr p) {
  return __builtin_amdgcn_ds_read_tr16_b64_v4i16((__attribute__((address_space(3))) bf16x4*)p);
}
DI bf16x8 cat4(bf16x4 a, bf16x4 b) { return __builtin_shufflevector(a, b, 0, 1, 2, 3, 4, 5, 6, 7); }
#define WAIT_BAR(N) asm volatile("s_waitcnt vmcnt(" #N ") lgkmcnt(0)\n\ts_barrier" ::: "memory")

constexpr int P_XB = 16384, P_WB = 20480, P_EB = 3072, P_STAGE = P_XB + P_WB + P_EB, P_NST = 4;
constexpr int P_GATE = P_STAGE * P_NST, P_LDS = P_GATE + 2048;

__global__ __launch_bounds__(512, 1) void k1_proj(const u16* __restrict__ x, const u16* __restrict__ wqkv, const u16* __restrict__ wq,
                                                  const u16* __restrict__ wk, const u16* __restrict__ wv, const u16* __restrict__ gw,
                                                  const u16* __restrict__ woa, const u16* __restrict__ wob, char* __restrict__ ws) {
  __shared__ __attribute__((aligned(16))) char lds[P_LDS];
  STAMP_DECL STAMP(0);
  const int tid = threadIdx.x, lane = tid & 63, r = lane & 31, h = lane >> 5;
  const int wid = __builtin_amdgcn_readfirstlane(tid >> 6);
  const int xcd = blockIdx.x & 7, bi = blockIdx.x >> 3;
  const int mt = (xcd & 1) * 8 + (bi & 7), j = (xcd >> 1) * 4 + (bi >> 3);
  const int m0 = mt * 128;
  const int typ = j >> 2;
  const bool wg_extra = typ == 0 || typ == 3;
  const int ex_rt = j & 3, ex_typ = typ == 3;
  const unsigned lds0 = (unsigned)(uintptr_t)lds;
  const lds_cptr L = (lds_cptr)lds;
  const bool vtype = typ == 2;
  const int rp = wid & 1, grp = wid >> 1;
  const bool my_extra = wg_extra && grp == 1 && rp == (ex_rt >> 1);
  const int rta = 2 * rp + (ex_rt & 1), rtb = 2 * rp + 1 - (ex_rt & 1);
  f32x16 acc[2][3] = {};

  if (wid >= 4) {
    const int lw = wid - 4;
    unsigned xoff[2], woff[5];
#pragma unroll
    for (int i = 0; i < 2; ++i) {
      const int row = lw * 32 + i * 8 + (lane >> 3), c = (lane & 7) ^ ((row >> 1) & 7);
      xoff[i] = (unsigned)(m0 + row) * 1280u + c * 16;
    }
    const u16* wsrc = (typ < 3) ? (wqkv + j * 160) : (wq + (j - 12) * 160);
    const unsigned ldw = (typ < 3) ? 3840u : 1280u;
    const u16* esrc = ex_typ ? wv : wk;
#pragma unroll
    for (int i = 0; i < 5; ++i) {
      const int idx = i * 64 + lane, row = (idx * 205) >> 12, gr = idx - row * 20;
      woff[i] = (unsigned)(lw * 16 + row) * ldw + gr * 16;
    }
    auto stage = [&](int s) {
      const unsigned dst = lds0 + (s & 3) * P_STAGE;
      const char* xb = (const char*)x + s * 128;
      const char* wb = (const char*)wsrc + (size_t)s * 64 * ldw;
#pragma unroll
      for (int i = 0; i < 4; ++i) glds16(xb + (i >> 1) * 16 * 1280, xoff[i & 1], dst + (lw * 4 + i) * 1024);
#pragma unroll
      for (int i = 0; i < 5; ++i) glds16(wb, woff[i], dst + P_XB + (lw * 5 + i) * 1024);
      if (wg_extra && lw < 3) {
        if (lw < 2 || lane < 32) glds16((const char*)esrc + s * 2560 + lw * 1024, lane * 16, dst + P_XB + P_WB + lw * 1024);
      }
    };
    if (typ == 0 && lw == 3) {
      glds16(gw, lane * 16, lds0 + P_GATE);
      if (lane < 16) glds16((const char*)gw + 1024, lane * 16, lds0 + P_GATE + 1024);
    }
    stage(0); stage(1);
    WAIT_BAR(9);
    stage(2);
    WAIT_BAR(9);
    stage(3);
#pragma unroll 1
    for (int s = 2; s < 10; ++s) {
      if (s + 2 < 10) stage(s + 2);
      if (s <= 7) WAIT_BAR(18); else if (s == 8) WAIT_BAR(9); else WAIT_BAR(0);
    }
    if (blockIdx.x < 8 && lw == 3) *(u32x4*)(ws + WS_F + blockIdx.x * 1024 + lane * 16) = (u32x4){0u, 0u, 0u, 0u};
#pragma unroll
    for (int it = 0; it < 2; ++it) {
      const int tl = it * 256 + tid - 256;
      if (tl < 400) {
        const int task = blockIdx.x * 400 + tl;
        u16 wv8[8];
        const int gr = task / 640, n = task - gr * 640;
        const bool mainp = gr < 128;
        const int hd0 = mainp ? (gr >> 1) : 2 * (gr - 128);
        const u16* w = (hd0 < 32 ? woa + hd0 * 20 * 640 : wob + (hd0 - 32) * 20 * 640) + (mainp ? 4 * (gr & 1) : 16) * 640 + n;
        const int q2 = mainp ? 8 * 640 : 20 * 640;
#pragma unroll
        for (int jj = 0; jj < 4; ++jj) { wv8[jj] = w[jj * 640]; wv8[4 + jj] = w[q2 + jj * 640]; }
        *(u32x4*)(ws + WS_W + (size_t)task * 16) = (u32x4){wv8[0] | ((unsigned)wv8[1] << 16), wv8[2] | ((unsigned)wv8[3] << 16),
                                                           wv8[4] | ((unsigned)wv8[5] << 16), wv8[6] | ((unsigned)wv8[7] << 16)};
      }
    }
  } else {
    const int xsw = h ^ ((r >> 1) & 7);
    const int xa = (rta * 32 + r) * 128, xb = (rtb * 32 + r) * 128;
    const int q4 = (lane & 15) >> 2, p4 = lane & 3, b16 = (lane >> 4) & 1;
    const int wbase = P_XB + (8 * h + q4) * 320 + (16 * b16 + 4 * p4) * 2 + grp * 192;
    const int ebase = P_XB + P_WB + (8 * h + q4) * 40 + (16 * b16 + 4 * p4) * 2;
    auto mainloop = [&](auto vt_, auto mode_) {
      constexpr bool VT = decltype(vt_)::value;
      constexpr int MODE = decltype(mode_)::value;
      struct Frag { bf16x8 xa, xb, w0, w1, w2, g; };
      auto rd = [&](Frag& f, int s, int ks) {
        const lds_cptr st = L + (s & 3) * P_STAGE;
        const lds_cptr wp = st + wbase + ks * 16 * 320;
        f.xa = lds_b128(st + xa + ((2 * ks) ^ xsw) * 16);
        f.w0 = cat4(lds_tr(wp), lds_tr(wp + 4 * 320));
        f.xb = lds_b128(st + xb + ((2 * ks) ^ xsw) * 16);
        f.w1 = cat4(lds_tr(wp + 64), lds_tr(wp + 64 + 4 * 320));
        if constexpr (MODE == 0) f.w2 = cat4(lds_tr(wp + 128), lds_tr(wp + 128 + 4 * 320));
        if constexpr (MODE >= 2) {
          const lds_cptr ep = st + ebase + ks * 16 * 40;
          f.w2 = cat4(lds_tr(ep), lds_tr(ep + 4 * 40));
          if constexpr (MODE == 2) f.g = lds_b128(L + P_GATE + (s * 64 + ks * 16 + 8 * h) * 2);
        }
      };
      auto mm = [&](f32x16& a, const bf16x8& xf, const bf16x8& wf, auto v_) {
        if constexpr (decltype(v_)::value) a = MFMA32(xf, wf, a); else a = MFMA32(wf, xf, a);
      };
      auto mmA = [&](const Frag& f) {
        mm(acc[0][0], f.xa, f.w0, vt_); mm(acc[1][0], f.xb, f.w0, vt_); mm(acc[0][1], f.xa, f.w1, vt_);
        if constexpr (MODE == 0) { mm(acc[0][2], f.xa, f.w2, vt_); mm(acc[1][2], f.xb, f.w2, vt_); }
        if constexpr (MODE == 2) acc[0][2] = MFMA32(r == 20 ? f.g : f.w2, f.xa, acc[0][2]);
        if constexpr (MODE == 3) acc[0][2] = MFMA32(f.xa, f.w2, acc[0][2]);
      };
      auto mmB = [&](const Frag& f) { mm(acc[1][1], f.xb, f.w1, vt_); };
      auto ilv = [&]() {
#pragma unroll
        for (int i = 0; i < (MODE == 0 ? 6 : (MODE == 1 ? 4 : 5)); ++i) {
          __builtin_amdgcn_sched_group_barrier(0x008, 1, 0);
          __builtin_amdgcn_sched_group_barrier(0x100, 2, 0);
        }
      };
      Frag f0, f1;
      asm volatile("s_barrier" ::: "memory");
      rd(f0, 0, 0);
      SBAR();
#pragma unroll 1
      for (int s = 0; s < 9; ++s) {
        rd(f1, s, 1); mmA(f0); mmB(f0); ilv(); SBAR();
        rd(f0, s, 2); mmA(f1); mmB(f1); ilv(); SBAR();
        rd(f1, s, 3); mmA(f0); mmB(f0); ilv(); SBAR();
        mmA(f1); SBAR();
        asm volatile("s_barrier" ::: "memory");
        mmB(f1); SBAR();
        rd(f0, s + 1, 0); SBAR();
      }
      rd(f1, 9, 1); mmA(f0); mmB(f0); ilv(); SBAR();
      rd(f0, 9, 2); mmA(f1); mmB(f1); ilv(); SBAR();
      rd(f1, 9, 3); mmA(f0); mmB(f0); ilv(); SBAR();
      mmA(f1); mmB(f1);
    };
    using std::integral_constant;
    if (vtype) {
      if (grp == 0) mainloop(integral_constant<bool, true>{}, integral_constant<int, 0>{});
      else          mainloop(integral_constant<bool, true>{}, integral_constant<int, 1>{});
    } else if (grp == 0) mainloop(integral_constant<bool, false>{}, integral_constant<int, 0>{});
    else if (!my_extra)  mainloop(integral_constant<bool, false>{}, integral_constant<int, 1>{});
    else if (ex_typ == 0) mainloop(integral_constant<bool, false>{}, integral_constant<int, 2>{});
    else                  mainloop(integral_constant<bool, false>{}, integral_constant<int, 3>{});
  }
  STAMP(2);

  const int ntile = grp == 0 ? 3 : 2;
  if (!vtype) {
    char* base = ws + (typ == 1 ? WS_K + (size_t)(j - 4) * 8 * HEAD_B : WS_Q + (size_t)(typ == 0 ? j : j - 8) * 8 * HEAD_B) + (size_t)m0 * 48 + r * 8;
    if (wid < 4) {
      const f32x2 sc = {typ == 1 ? 1.0f : QSCALE, typ == 1 ? 1.0f : QSCALE};
#pragma unroll
      for (int t = 0; t < 3; ++t) {
        if (t < ntile) {
#pragma unroll
          for (int g = 0; g < 4; ++g) {
            const int qd = 8 * (grp * 3 + t) + 2 * g + h, hl = (qd * 205) >> 10, dq = qd - hl * 5;
            const unsigned off = (unsigned)hl * (unsigned)HEAD_B + dq * 256;
#pragma unroll
            for (int i = 0; i < 2; ++i) {
              const f32x16& a = acc[i][t];
              const f32x2 lo = (f32x2){a[4 * g], a[4 * g + 1]} * sc, hi = (f32x2){a[4 * g + 2], a[4 * g + 3]} * sc;
              *(u32x2*)(base + (i ? rtb : rta) * 1536 + off) = (u32x2){cvtpk(lo[0], lo[1]), cvtpk(hi[0], hi[1])};
            }
          }
        }
      }
    } else if (typ == 1) {
#pragma unroll
      for (int i = 0; i < 4; ++i) *(u32x2*)(base + (wid - 4) * 1536 + (size_t)(2 * i + h) * HEAD_B + 5 * 256) = (u32x2){0x3F80u, 0u};
    }
  } else {
    char* base = ws + WS_V + (size_t)(j - 8) * 8 * HEAD_B + (size_t)(m0 / 16) * 768 + h * 384;
    if (wid < 4) {
#pragma unroll
      for (int t = 0; t < 3; ++t) {
        if (t < ntile) {
          const int n = 32 * (grp * 3 + t) + r, hl = (n * 205) >> 12, d = n - hl * 20;
#pragma unroll
          for (int i = 0; i < 2; ++i) {
            const f32x16& a = acc[i][t];
#pragma unroll
            for (int sp = 0; sp < 2; ++sp) {
              u32x4 v = {cvtpk(a[8 * sp], a[8 * sp + 1]), cvtpk(a[8 * sp + 2], a[8 * sp + 3]),
                         cvtpk(a[8 * sp + 4], a[8 * sp + 5]), cvtpk(a[8 * sp + 6], a[8 * sp + 7])};
              *(u32x4*)(base + (size_t)hl * HEAD_B + (i ? rtb : rta) * 1536 + sp * 768 + d * 16) = v;
            }
          }
        }
      }
    } else {
      const unsigned cv = (r & 3) == 0 ? 0x3F803F80u : 0u;
#pragma unroll
      for (int sp = 0; sp < 2; ++sp)
        *(u32x4*)(base + (size_t)(r >> 2) * HEAD_B + (wid - 4) * 1536 + sp * 768 + (20 + (r & 3)) * 16) = (u32x4){cv, cv, cv, cv};
    }
  }
  if (my_extra) {
    const f32x16& e = acc[0][2];
    const int tok = m0 + rta * 32 + r;
    if (ex_typ == 0) {
      char* base = ws + WS_K + (size_t)32 * HEAD_B + (size_t)(m0 + rta * 32) * 48 + r * 8;
#pragma unroll
      for (int g = 0; g < 3; ++g) {
        u32x2 v = {cvtpk(e[4 * g], e[4 * g + 1]), cvtpk(e[4 * g + 2], e[4 * g + 3])};
        if (g == 2 && h == 1) v = (u32x2){0x3F80u, 0u};
        *(u32x2*)(base + (2 * g + h) * 256) = v;
      }
      if (h == 1) *(float*)(ws + WS_G + (size_t)tok * 4) = 1.0f / (1.0f + __expf(-e[8]));
    } else if (r < 24) {
      char* base = ws + WS_V + (size_t)32 * HEAD_B + (size_t)((m0 + rta * 32) / 16) * 768 + h * 384 + r * 16;
      const unsigned cv = r == 20 ? 0x3F803F80u : 0u;
#pragma unroll
      for (int sp = 0; sp < 2; ++sp) {
        u32x4 v = {cvtpk(e[8 * sp], e[8 * sp + 1]), cvtpk(e[8 * sp + 2], e[8 * sp + 3]),
                   cvtpk(e[8 * sp + 4], e[8 * sp + 5]), cvtpk(e[8 * sp + 6], e[8 * sp + 7])};
        if (r >= 20) v = (u32x4){cv, cv, cv, cv};
        *(u32x4*)(base + sp * 768) = v;
      }
    }
  }
  STAMP(3);
  STAMP_OUT(0, ws);
}

constexpr int A_CHUNK = 24576, A_KB = 12288, A_NSLOT = 3, A_LDS = 20 * 7 * 1024;

DI float half_max(float m) {
  auto rr = __builtin_amdgcn_permlane32_swap(__float_as_uint(m), __float_as_uint(m), false, false);
  return fmaxf(__uint_as_float(rr[0]), __uint_as_float(rr[1]));
}
DI float max16(const f32x16& s) {
  float a = fmaxf(fmaxf(s[0], s[1]), s[2]), b = fmaxf(fmaxf(s[3], s[4]), s[5]);
  a = fmaxf(fmaxf(a, s[6]), s[7]); b = fmaxf(fmaxf(b, s[8]), s[9]);
  a = fmaxf(fmaxf(a, s[10]), s[11]); b = fmaxf(fmaxf(b, s[12]), s[13]);
  a = fmaxf(fmaxf(a, s[14]), s[15]);
  return half_max(fmaxf(a, b));
}
DI f32x16 attn_safe(const char* kg, const char* vg, bf16x8 q1, bf16x4 q2, int r, int h) {
  if (h) q2 = (bf16x4){0, 0, 0, 0};
  f32x16 o = {};
  float m = -INFINITY;
#pragma unroll 1
  for (int t = 0; t < 64; ++t) {
    const char* kp = kg + t * 1536 + r * 8;
    const bf16x8 k1 = cat4(*(const bf16x4*)(kp + h * 512), *(const bf16x4*)(kp + h * 512 + 256));
    const bf16x4 k2 = *(const bf16x4*)(kp + 1024 + h * 256);
    const int vr = r < 24 ? r : 23;
    const bf16x8 v0 = *(const bf16x8*)(vg + t * 1536 + h * 384 + vr * 16);
    const bf16x8 v1 = *(const bf16x8*)(vg + t * 1536 + 768 + h * 384 + vr * 16);
    f32x16 s = {};
    s = MFMA32(k1, q1, s);
    s = MFMA8(k2, q2, s);
    const float mn = fmaxf(m, max16(s));
    const float al = __builtin_amdgcn_exp2f(m - mn);
    m = mn;
#pragma unroll
    for (int i = 0; i < 16; ++i) { o[i] *= al; s[i] = __builtin_amdgcn_exp2f(s[i] - mn); }
    u32x4 p0 = {cvtpk(s[0], s[1]), cvtpk(s[2], s[3]), cvtpk(s[4], s[5]), cvtpk(s[6], s[7])};
    u32x4 p1 = {cvtpk(s[8], s[9]), cvtpk(s[10], s[11]), cvtpk(s[12], s[13]), cvtpk(s[14], s[15])};
    o = MFMA32(v0, __builtin_bit_cast(bf16x8, p0), o);
    o = MFMA32(v1, __builtin_bit_cast(bf16x8, p1), o);
  }
  return o;
}
DI bool o_unsafe(const f32x16& o, int h) {
  float a = fabsf(o[0]) + fabsf(o[1]), b = fabsf(o[2]) + fabsf(o[3]);
#pragma unroll
  for (int i = 4; i < 8; i += 2) { a += fabsf(o[i]); b += fabsf(o[i + 1]); }
  float c = fabsf(o[8]) + fabsf(o[9]) + fabsf(o[10]) + fabsf(o[11]);
  if (h) c = (o[8] < 1.0e18f && o[8] > 1.0e-18f) ? 0.f : INFINITY;
  return !(a + b + c < 3.0e38f);
}
DI void attn_store(const f32x16& o, __amdgpu_buffer_rsrc_t og, int hh, int tok, float gfac, int h) {
  auto rr = __builtin_amdgcn_permlane32_swap(__float_as_uint(o[8]), __float_as_uint(o[8]), false, false);
  const float sc = gfac * __builtin_amdgcn_rcpf(__uint_as_float(rr[1]));
  u32x4 v = {cvtpk(o[0] * sc, o[1] * sc), cvtpk(o[2] * sc, o[3] * sc), cvtpk(o[4] * sc, o[5] * sc), cvtpk(o[6] * sc, o[7] * sc)};
  __builtin_amdgcn_raw_buffer_store_b128(v, og, ((hh * 2 + h) * S + tok) * 16, 0, 16);
  if (h == 0) {
    u32x2 t = {cvtpk(o[8] * sc, o[9] * sc), cvtpk(o[10] * sc, o[11] * sc)};
    __builtin_amdgcn_raw_buffer_store_b64(t, og, ((128 + (hh >> 1)) * S + tok) * 16 + (hh & 1) * 8, 0, 16);
  }
}
typedef __attribute__((address_space(1))) unsigned gu32;

__global__ __launch_bounds__(512, 1) void k2_attn(char* __restrict__ ws, u16* __restrict__ out) {
  __shared__ __attribute__((aligned(16))) char lds[A_LDS];
  STAMP_DECL STAMP(0);
  const int tid = threadIdx.x, lane = tid & 63, r = lane & 31, h = lane >> 5;
  const int wid = __builtin_amdgcn_readfirstlane(tid >> 6);
  const int xcd = blockIdx.x & 7, bi = blockIdx.x >> 3;
  const int hh = (bi >> 4) * 32 + xcd * 4 + ((bi >> 2) & 3), qb = bi & 3;
  const int kvh = hh < 32 ? hh : 32;
  const char* kg = ws + WS_K + (size_t)kvh * HEAD_B;
  const char* vg = ws + WS_V + (size_t)kvh * HEAD_B;
  const int tokA = qb * 512 + wid * 64 + r, tokB = tokA + 32;
  const unsigned lds0 = (unsigned)(uintptr_t)lds;
  const lds_cptr L = (lds_cptr)lds;

  const char* qg = ws + WS_Q + (size_t)hh * HEAD_B;
  const char* qp = qg + (tokA - r) * 48 + r * 8;
  const bf16x8 qA1 = cat4(*(const bf16x4*)(qp + h * 512), *(const bf16x4*)(qp + h * 512 + 256));
  const bf16x8 qB1 = cat4(*(const bf16x4*)(qp + 1536 + h * 512), *(const bf16x4*)(qp + 1536 + h * 512 + 256));
  bf16x4 qA2 = *(const bf16x4*)(qp + 1024), qB2 = *(const bf16x4*)(qp + 1536 + 1024);

  const float gA = *(const float*)(ws + WS_G + tokA * 4), gB = *(const float*)(ws + WS_G + tokB * 4);

  const char* dsrc = wid < 4 ? kg + wid * 3072 : vg + (wid - 4) * 3072;
  auto dma1 = [&](int c, int slot, int i) { glds16(dsrc + c * A_KB + i * 1024, lane * 16, lds0 + slot * A_CHUNK + (wid * 3 + i) * 1024); };
  auto dma = [&](int c, int slot) { dma1(c, slot, 0); dma1(c, slot, 1); dma1(c, slot, 2); };
  dma(0, 0);
  WAIT_BAR(0);
  dma(1, 1);
  const int mt = (xcd >> 1) * 8 + (bi & 7), nb = (xcd & 1) * 4 + (bi >> 3);
  const int c16 = lane & 15, g = lane >> 4;
  bf16x8 wf[5][5], of[5][4];
  const unsigned wvo = (g * D + c16) * 16;
  const char* wgs = ws + WS_W + ((size_t)wid * 20 * D + nb * 80) * 16;
#define WLOAD(c, n) do { if ((c) < 5) asm volatile("global_load_dwordx4 %0, %1, %2 offset:%c3" : "=v"(wf[(c) < 5 ? (c) : 0][n]) \
      : "v"(wvo), "s"(wgs + (c) * (4 * D * 16)), "i"((n) * 256) : "memory"); } while (0)

  const int kb1 = r * 8 + h * 512, kb2 = r * 8 + 1024 + h * 256, vb = A_KB + h * 384 + (r < 21 ? r : 21) * 16;
  int kb1b = kb1 + 256; PIN(kb1b);
  bf16x8 k1a = cat4(lds_b64(L + kb1), lds_b64(L + kb1b)), k1b;
  bf16x4 k2a = lds_b64(L + kb2), k2b;
  bf16x8 v0a = {}, v1a = {}, v0b = {}, v1b = {};
  const bf16x4 q2keepA = qA2, q2keepB = qB2;
  f32x16 sA, sB, oA = {}, oB = {};
  const f32x16 zero = {};
  {
    if (h) { qA2 = (bf16x4){0, 0, 0, 0}; qB2 = (bf16x4){0, 0, 0, 0}; }
    sA = MFMA32(k1a, qA1, zero); sB = MFMA32(k1a, qB1, zero);
    sA = MFMA8(k2a, qA2, sA);    sB = MFMA8(k2a, qB2, sB);
    const unsigned ma = cvtpk(-max16(sA), 0.f), mb = cvtpk(-max16(sB), 0.f);
    if (h) { qA2 = __builtin_bit_cast(bf16x4, (u32x2){ma, 0u}); qB2 = __builtin_bit_cast(bf16x4, (u32x2){mb, 0u}); }
    sA = MFMA32(k1a, qA1, zero);
    sA = MFMA8(k2a, qA2, sA);
  }
  u32x4 pA0 = {}, pA1 = {}, pB0 = {}, pB1 = {};

#define PRIO(n) __builtin_amdgcn_s_setprio(n)
#define EX(v) __builtin_amdgcn_exp2f(v)
#define GAP(MF, X, i, P, e) do { MF; SBAR(); X[i] = EX(X[i]); X[i + 1] = EX(X[i + 1]); X[i + 2] = EX(X[i + 2]); X[i + 3] = EX(X[i + 3]); \
    P[e] = cvtpk(X[i], X[i + 1]); P[e + 1] = cvtpk(X[i + 2], X[i + 3]); PIN(P); SBAR(); } while (0)
#define BF(p) __builtin_bit_cast(bf16x8, p)
#define PHASE(X, PX0, PX1, Y, OY, PY0, PY1, QY1, QY2, K1, K2, V0, V1) do { \
    GAP(Y = MFMA32(K1, QY1, zero), X, 0, PX0, 0);  \
    GAP(Y = MFMA8(K2, QY2, Y),     X, 4, PX0, 2);  \
    GAP(OY = MFMA32(V0, BF(PY0), OY), X, 8, PX1, 0);  \
    GAP(OY = MFMA32(V1, BF(PY1), OY), X, 12, PX1, 2); } while (0)
#define TILE(KP, VP, K1C, K2C, V0C, V1C, K1N, K2N, V0N, V1N) do { \
    K1N = cat4(lds_b64((KP) + kb1), lds_b64((KP) + kb1b)); K2N = lds_b64((KP) + kb2); V0N = lds_b128((VP) + vb); V1N = lds_b128((VP) + vb + 768); SBAR(); \
    PHASE(sA, pA0, pA1, sB, oB, pB0, pB1, qB1, qB2, K1C, K2C, V0C, V1C); \
    __builtin_amdgcn_s_waitcnt(0xC07F); SBAR(); \
    PHASE(sB, pB0, pB1, sA, oA, pA0, pA1, qA1, qA2, K1N, K2N, V0N, V1N); } while (0)

  STAMP(1);
#pragma unroll
  for (int c = 0; c < 8; ++c) {
    const int slot = c % A_NSLOT, nslot = (c + 1) % A_NSLOT;
    const lds_cptr cp = L + slot * A_CHUNK, np = L + nslot * A_CHUNK;
    const bool mid = c >= 1 && c < 7;
    PRIO(3);
    if (mid) dma1(c + 1, nslot, 1);
    TILE(cp + 1 * 1536, cp + 0 * 1536, k1a, k2a, v0a, v1a, k1b, k2b, v0b, v1b);
    if (mid) dma1(c + 1, nslot, 2);
    TILE(cp + 2 * 1536, cp + 1 * 1536, k1b, k2b, v0b, v1b, k1a, k2a, v0a, v1a);
    PRIO(2);
    WLOAD(c, 0);
    TILE(cp + 3 * 1536, cp + 2 * 1536, k1a, k2a, v0a, v1a, k1b, k2b, v0b, v1b);
    WLOAD(c, 1);
    TILE(cp + 4 * 1536, cp + 3 * 1536, k1b, k2b, v0b, v1b, k1a, k2a, v0a, v1a);
    PRIO(1);
    WLOAD(c, 2);
    TILE(cp + 5 * 1536, cp + 4 * 1536, k1a, k2a, v0a, v1a, k1b, k2b, v0b, v1b);
    WLOAD(c, 3);
    TILE(cp + 6 * 1536, cp + 5 * 1536, k1b, k2b, v0b, v1b, k1a, k2a, v0a, v1a);
    PRIO(0);
    WLOAD(c, 4);
    TILE(cp + 7 * 1536, cp + 6 * 1536, k1a, k2a, v0a, v1a, k1b, k2b, v0b, v1b);
    if (c < 7) WAIT_BAR(0);
    PRIO(3);
    if (c + 2 < 8) dma1(c + 2, (c + 2) % A_NSLOT, 0);
    TILE(np, cp + 7 * 1536, k1b, k2b, v0b, v1b, k1a, k2a, v0a, v1a);
  }
  STAMP(2);
  oB = MFMA32(v0a, BF(pB0), oB);
  oB = MFMA32(v1a, BF(pB1), oB);

  if (__builtin_expect(__any(o_unsafe(oA, h) || o_unsafe(oB, h)), 0)) {
    oA = attn_safe(kg, vg, qA1, q2keepA, r, h);
    oB = attn_safe(kg, vg, qB1, q2keepB, r, h);
  }
  const __amdgpu_buffer_rsrc_t og = __builtin_amdgcn_make_buffer_rsrc(ws + WS_O, 0, 160 * S * 16, 0x00020000);
  attn_store(oA, og, hh, tokA, hh < 32 ? gA : 1.0f - gA, h);
  attn_store(oB, og, hh, tokB, hh < 32 ? gB : 1.0f - gB, h);
  SBAR();

  asm volatile("s_waitcnt vmcnt(0) lgkmcnt(0)\n\ts_barrier" ::: "memory");
  STAMP(3);
  gu32* flags = (gu32*)(ws + WS_F);
  if (wid == 0) {
    if (lane < 8) __hip_atomic_store(flags + (qb * 8 + lane) * 64 + hh, 1u, __ATOMIC_RELAXED, __HIP_MEMORY_SCOPE_AGENT);
    gu32* pf = flags + ((mt >> 3) * 8 + nb) * 64 + lane;
    for (unsigned spins = 0; spins < (1u << 22); ++spins) {
      if (__all(__hip_atomic_load(pf, __ATOMIC_RELAXED, __HIP_MEMORY_SCOPE_AGENT) == 1u)) break;
      __builtin_amdgcn_s_sleep(1);
    }
  }
  asm volatile("s_barrier" ::: "memory");
  STAMP(4);
  const int ob = ((wid * 20 + g) * S + mt * 64 + c16) * 16;
  auto oload = [&](int s) {
#pragma unroll
    for (int m = 0; m < 4; ++m)
      of[s][m] = __builtin_bit_cast(bf16x8, __builtin_amdgcn_raw_buffer_load_b128(og, ob, s * 4 * S * 16 + ((m + wid) & 3) * 256, 16));
  };
#pragma unroll
  for (int s = 0; s < 4; ++s) oload(s);
  SBAR();
  f32x4 acc[4][5] = {};
#pragma unroll
  for (int s = 0; s < 5; ++s) {
#pragma unroll
    for (int m = 3; m >= 0; --m)
#pragma unroll
      for (int n = 0; n < 5; ++n) acc[m][n] = MFMA16(wf[s][n], of[s][m], acc[m][n]);
    SBAR();
    if (s == 0) oload(4);
    SBAR();
  }
  const int nh = wid >> 2;
#pragma unroll
  for (int m = 3; m >= 0; --m)
#pragma unroll
    for (int n = 0; n < 5; ++n) {
      const int gm = (m + wid) & 3, owner = gm + (n >= 3 ? 4 : 0), sl = (wid - owner) & 7;
      if (m != 0 || (n >= 3) != (nh == 1)) *(f32x4*)(lds + ((gm * 5 + n) * 7 + sl - 1) * 1024 + lane * 16) = acc[m][n];
    }
  __syncthreads();
  u16* op = out + (size_t)(mt * 64 + (wid & 3) * 16 + c16) * D + nb * 80 + g * 4;
#pragma unroll
  for (int n = 0; n < 5; ++n) {
    if ((n >= 3) == (nh == 1)) {
      f32x4 a = acc[0][n];
#pragma unroll
      for (int sl = 0; sl < 7; ++sl) a += *(const f32x4*)(lds + (((wid & 3) * 5 + n) * 7 + sl) * 1024 + lane * 16);
      *(u32x2*)(op + n * 16) = (u32x2){cvtpk(a[0], a[1]), cvtpk(a[2], a[3])};
    }
  }
  STAMP_OUT(1, ws);
}

extern "C" void kernel_launch(void* const* d_in, const int* in_sizes, int n_in, void* d_out, int out_size, void* d_ws, size_t ws_size,
                              hipStream_t stream) {
  const u16* x = (const u16*)d_in[0];
  const u16* wqkv = (const u16*)d_in[1];
  const u16* woa = (const u16*)d_in[2];
  const u16* wq = (const u16*)d_in[4];
  const u16* wk = (const u16*)d_in[5];
  const u16* wv = (const u16*)d_in[6];
  const u16* wob = (const u16*)d_in[7];
  const u16* gw = (const u16*)d_in[8];
  char* ws = (char*)d_ws;
  k1_proj<<<256, 512, 0, stream>>>(x, wqkv, wq, wk, wv, gw, woa, wob, ws);
  k2_attn<<<256, 512, 0, stream>>>(ws, (u16*)d_out);
}
```

```cpp
#include <hip/hip_runtime.h>
#include <hip/hip_bf16.h>
#include <stdint.h>
#include <type_traits>

typedef __attribute__((ext_vector_type(8))) short bf16x8;
typedef __attribute__((ext_vector_type(4))) short bf16x4;
typedef __attribute__((ext_vector_type(16))) float f32x16;
typedef __attribute__((ext_vector_type(4))) float f32x4;
typedef __attribute__((ext_vector_type(2))) float f32x2;
typedef __attribute__((ext_vector_type(4))) unsigned u32x4;
typedef __attribute__((ext_vector_type(2))) unsigned u32x2;
typedef __attribute__((ext_vector_type(2))) __bf16 bf16x2_t;
typedef __attribute__((address_space(3))) const char* lds_cptr;
typedef unsigned short u16;

#define DI __device__ __forceinline__
#define SBAR() __builtin_amdgcn_sched_barrier(0)
#define PIN(x) asm volatile("" : "+v"(x))
#define MFMA32(a, b, c) __builtin_amdgcn_mfma_f32_32x32x16_bf16((a), (b), (c), 0, 0, 0)
#define MFMA8(a, b, c) __builtin_amdgcn_mfma_f32_32x32x8bf16_1k((a), (b), (c), 0, 0, 0)
#define MFMA16(a, b, c) __builtin_amdgcn_mfma_f32_16x16x32_bf16((a), (b), (c), 0, 0, 0)

#define STAMP_DECL
#define STAMP(i)
#define STAMP_OUT(kid, wsb)

constexpr int S = 2048, D = 640;
constexpr float QSCALE = 0.22360679774997896f * 1.4426950408889634f;

constexpr size_t HEAD_B = (size_t)S * 48;
constexpr size_t WS_Q = 0;
constexpr size_t WS_K = WS_Q + 64 * HEAD_B;
constexpr size_t WS_V = WS_K + 33 * HEAD_B;
constexpr size_t WS_G = WS_V + 33 * HEAD_B;
constexpr size_t WS_O = WS_G + S * 4;
constexpr size_t WS_W = WS_O + (size_t)160 * S * 16;
constexpr size_t WS_F = WS_W + (size_t)160 * D * 16;
constexpr size_t WS_END = WS_F + 8192;

DI unsigned cvtpk(float lo, float hi) {
  f32x2 v = {lo, hi};
  return __builtin_bit_cast(unsigned, __builtin_convertvector(v, bf16x2_t));
}
DI void glds16(const void* sbase, unsigned voff, unsigned lds_dst) {
  unsigned keep;
  asm volatile("s_mov_b32 %0, m0\n\ts_mov_b32 m0, %3\n\ts_nop 0\n\tglobal_load_lds_dwordx4 %1, %2\n\ts_mov_b32 m0, %0"
               : "=&s"(keep) : "v"(voff), "s"(sbase), "s"(lds_dst) : "memory");
}
DI bf16x8 lds_b128(lds_cptr p) { return *(const __attribute__((address_space(3))) bf16x8*)p; }
DI bf16x4 lds_b64(lds_cptr p) { return *(const __attribute__((address_space(3))) bf16x4*)p; }
DI bf16x4 lds_tr(lds_cptr p) {
  return __builtin_amdgcn_ds_read_tr16_b64_v4i16((__attribute__((address_space(3))) bf16x4*)p);
}
DI bf16x8 cat4(bf16x4 a, bf16x4 b) { return __builtin_shufflevector(a, b, 0, 1, 2, 3, 4, 5, 6, 7); }
#define WAIT_BAR(N) asm volatile("s_waitcnt vmcnt(" #N ") lgkmcnt(0)\n\ts_barrier" ::: "memory")

constexpr int P_XB = 16384, P_WB = 20480, P_EB = 3072, P_STAGE = P_XB + P_WB + P_EB, P_NST = 4;
constexpr int P_GATE = P_STAGE * P_NST, P_LDS = P_GATE + 2048;

__global__ __launch_bounds__(768, 1) void k1_proj(const u16* __restrict__ x, const u16* __restrict__ wqkv, const u16* __restrict__ wq,
                                                  const u16* __restrict__ wk, const u16* __restrict__ wv, const u16* __restrict__ gw,
                                                  const u16* __restrict__ woa, const u16* __restrict__ wob, char* __restrict__ ws,
                                                  const u16* __restrict__ out) {
  __shared__ __attribute__((aligned(16))) char lds[P_LDS];
  STAMP_DECL STAMP(0);
  const int tid = threadIdx.x, lane = tid & 63, r = lane & 31, h = lane >> 5;
  const int wid = __builtin_amdgcn_readfirstlane(tid >> 6);
  const int xcd = blockIdx.x & 7, bi = blockIdx.x >> 3;
  const int mt = (xcd & 1) * 8 + (bi & 7), j = (xcd >> 1) * 4 + (bi >> 3);
  const int m0 = mt * 128;
  const int typ = j >> 2;
  const bool wg_extra = typ == 0 || typ == 3;
  const int ex_rt = j & 3, ex_typ = typ == 3;
  const unsigned lds0 = (unsigned)(uintptr_t)lds;
  const lds_cptr L = (lds_cptr)lds;
  const bool vtype = typ == 2;
  const int rt = wid & 3, grp = wid >> 2;
  const bool my_extra = !vtype && grp == 1 && rt == ex_rt;
  f32x16 acc[3] = {};

  if (grp == 2) {
    unsigned xoff[2], woff[5];
#pragma unroll
    for (int i = 0; i < 2; ++i) {
      const int row = rt * 32 + i * 8 + (lane >> 3), c = (lane & 7) ^ ((row >> 1) & 7);
      xoff[i] = (unsigned)(m0 + row) * 1280u + c * 16;
    }
    auto stage_x = [&](int s) {
#pragma unroll
      for (int i = 0; i < 4; ++i)
        glds16((const char*)x + s * 128 + (i >> 1) * 16 * 1280, xoff[i & 1], lds0 + (s & 3) * P_STAGE + (rt * 4 + i) * 1024);
    };
    stage_x(0);
    const u16* wsrc = (typ < 3) ? (wqkv + j * 160) : (wq + (j - 12) * 160);
    const unsigned ldw = (typ < 3) ? 3840u : 1280u;
    const u16* esrc = ex_typ ? wv : wk;
#pragma unroll
    for (int i = 0; i < 5; ++i) {
      const int idx = i * 64 + lane, row = (idx * 205) >> 12, gr = idx - row * 20;
      woff[i] = (unsigned)(rt * 16 + row) * ldw + gr * 16;
    }
    auto stage_w = [&](int s) {
      const unsigned dst = lds0 + (s & 3) * P_STAGE + P_XB;
      const char* wb = (const char*)wsrc + (size_t)s * 64 * ldw;
#pragma unroll
      for (int i = 0; i < 5; ++i) glds16(wb, woff[i], dst + (rt * 5 + i) * 1024);
      if (wg_extra && rt < 3) {
        if (rt < 2 || lane < 32) glds16((const char*)esrc + s * 2560 + rt * 1024, lane * 16, dst + P_WB + rt * 1024);
      }
    };
    auto stage = [&](int s) { stage_x(s); stage_w(s); };
    stage_w(0);
    if (typ == 1 && rt == 3) {
      glds16(gw, lane * 16, lds0 + P_GATE);
      if (lane < 16) glds16((const char*)gw + 1024, lane * 16, lds0 + P_GATE + 1024);
    }
    stage(1);
    WAIT_BAR(9);
    stage(2);
    WAIT_BAR(9);
    stage(3);
#pragma unroll 1
    for (int s = 2; s < 10; ++s) {
      if (s + 2 < 10) stage(s + 2);
      if (s <= 7) WAIT_BAR(18); else if (s == 8) WAIT_BAR(9); else WAIT_BAR(0);
    }
  } else {
    if (tid < 400) {
      const int task = blockIdx.x * 400 + tid;
      u16 wv8[8];
      const int gr = task / 640, n = task - gr * 640;
      const bool mainp = gr < 128;
      const int hd0 = mainp ? (gr >> 1) : 2 * (gr - 128);
      const u16* w = (hd0 < 32 ? woa + hd0 * 20 * 640 : wob + (hd0 - 32) * 20 * 640) + (mainp ? 4 * (gr & 1) : 16) * 640 + n;
      const int q2 = mainp ? 8 * 640 : 20 * 640;
#pragma unroll
      for (int jj = 0; jj < 4; ++jj) { wv8[jj] = w[jj * 640]; wv8[4 + jj] = w[q2 + jj * 640]; }
      *(u32x4*)(ws + WS_W + (size_t)task * 16) = (u32x4){wv8[0] | ((unsigned)wv8[1] << 16), wv8[2] | ((unsigned)wv8[3] << 16),
                                                         wv8[4] | ((unsigned)wv8[5] << 16), wv8[6] | ((unsigned)wv8[7] << 16)};
    }
    if (wid == 7) {
      if (blockIdx.x < 8) *(u32x4*)(ws + WS_F + blockIdx.x * 1024 + lane * 16) = (u32x4){0u, 0u, 0u, 0u};
      const char* pg = lane < 48 ? ws + (size_t)lane * ((WS_END / 48) & ~(size_t)255) : (const char*)out + (size_t)(lane - 48) * (S * D * 2 / 16);
      unsigned dummy;
      asm volatile("global_load_dword %0, %1, off\n\ts_waitcnt vmcnt(0)" : "=v"(dummy) : "v"(pg) : "memory");
    }
    const int xsw = h ^ ((r >> 1) & 7), xa = (rt * 32 + r) * 128;
    const int q4 = (lane & 15) >> 2, p4 = lane & 3, b16 = (lane >> 4) & 1;
    const int wbase = P_XB + (8 * h + q4) * 320 + (16 * b16 + 4 * p4) * 2 + grp * 192;
    const int ebase = P_XB + P_WB + (8 * h + q4) * 40 + (16 * b16 + 4 * p4) * 2;
    auto mainloop = [&](auto vt_, auto mode_) {
      constexpr bool VT = decltype(vt_)::value;
      constexpr int MODE = decltype(mode_)::value;
      struct Frag { bf16x8 x, w0, w1, w2; };
      auto rd = [&](Frag& f, int s, int ks) {
        const lds_cptr st = L + (s & 3) * P_STAGE;
        const lds_cptr wp = st + wbase + ks * 16 * 320;
        f.x = lds_b128(st + xa + ((2 * ks) ^ xsw) * 16);
        f.w0 = cat4(lds_tr(wp), lds_tr(wp + 4 * 320));
        f.w1 = cat4(lds_tr(wp + 64), lds_tr(wp + 64 + 4 * 320));
        if constexpr (MODE == 0) f.w2 = cat4(lds_tr(wp + 128), lds_tr(wp + 128 + 4 * 320));
        if constexpr (MODE == 2 || MODE == 3) {
          const lds_cptr ep = st + ebase + ks * 16 * 40;
          f.w2 = cat4(lds_tr(ep), lds_tr(ep + 4 * 40));
        }
        if constexpr (MODE == 4) f.w2 = lds_b128(L + P_GATE + (s * 64 + ks * 16 + 8 * h) * 2);
      };
      auto mm = [&](f32x16& a, const bf16x8& xf, const bf16x8& wf) {
        if constexpr (VT) a = MFMA32(xf, wf, a); else a = MFMA32(wf, xf, a);
      };
      auto mmA = [&](const Frag& f) {
        mm(acc[0], f.x, f.w0); mm(acc[1], f.x, f.w1);
        if constexpr (MODE == 0) mm(acc[2], f.x, f.w2);
        if constexpr (MODE == 2 || MODE == 4) acc[2] = MFMA32(f.w2, f.x, acc[2]);
        if constexpr (MODE == 3) acc[2] = MFMA32(f.x, f.w2, acc[2]);
      };
      auto ilv = [&]() {
#pragma unroll
        for (int i = 0; i < (MODE == 1 ? 2 : 3); ++i) {
          __builtin_amdgcn_sched_group_barrier(0x008, 1, 0);
          __builtin_amdgcn_sched_group_barrier(0x100, 3, 0);
        }
      };
      Frag f0, f1;
      asm volatile("s_barrier" ::: "memory");
      rd(f0, 0, 0);
      SBAR();
#pragma unroll 1
      for (int s = 0; s < 9; ++s) {
        rd(f1, s, 1); mmA(f0); ilv(); SBAR();
        rd(f0, s, 2); mmA(f1); ilv(); SBAR();
        rd(f1, s, 3); mmA(f0); ilv(); SBAR();
        mmA(f1); SBAR();
        asm volatile("s_barrier" ::: "memory");
        rd(f0, s + 1, 0); SBAR();
      }
      rd(f1, 9, 1); mmA(f0); ilv(); SBAR();
      rd(f0, 9, 2); mmA(f1); ilv(); SBAR();
      rd(f1, 9, 3); mmA(f0); ilv(); SBAR();
      mmA(f1);
    };
    using std::integral_constant;
    if (vtype) {
      if (grp == 0) mainloop(integral_constant<bool, true>{}, integral_constant<int, 0>{});
      else          mainloop(integral_constant<bool, true>{}, integral_constant<int, 1>{});
    } else if (grp == 0) mainloop(integral_constant<bool, false>{}, integral_constant<int, 0>{});
    else if (!my_extra)  mainloop(integral_constant<bool, false>{}, integral_constant<int, 1>{});
    else if (typ == 0)   mainloop(integral_constant<bool, false>{}, integral_constant<int, 2>{});
    else if (typ == 3)   mainloop(integral_constant<bool, false>{}, integral_constant<int, 3>{});
    else                 mainloop(integral_constant<bool, false>{}, integral_constant<int, 4>{});
  }
  STAMP(2);

  const int ntile = grp == 0 ? 3 : 2;
  if (!vtype) {
    char* base = ws + (typ == 1 ? WS_K + (size_t)(j - 4) * 8 * HEAD_B : WS_Q + (size_t)(typ == 0 ? j : j - 8) * 8 * HEAD_B) + (size_t)(m0 + rt * 32) * 48;
    if (grp < 2) {
      const f32x2 sc = {typ == 1 ? 1.0f : QSCALE, typ == 1 ? 1.0f : QSCALE};
#pragma unroll
      for (int t = 0; t < 3; ++t) {
        if (t < ntile) {
          const f32x16& a = acc[t];
#pragma unroll
          for (int g = 0; g < 4; ++g) {
            const int qd = 8 * (grp * 3 + t) + 2 * g + h, hl = (qd * 205) >> 10, dq = qd - hl * 5;
            const unsigned off = (unsigned)hl * (unsigned)HEAD_B + dq * 256 + r * 8;
            const f32x2 lo = (f32x2){a[4 * g], a[4 * g + 1]} * sc, hi = (f32x2){a[4 * g + 2], a[4 * g + 3]} * sc;
            *(u32x2*)(base + off) = (u32x2){cvtpk(lo[0], lo[1]), cvtpk(hi[0], hi[1])};
          }
        }
      }
    } else if (typ == 1) {
#pragma unroll
      for (int i = 0; i < 4; ++i) *(u32x2*)(base + (size_t)(2 * i + h) * HEAD_B + 5 * 256 + r * 8) = (u32x2){0x3F80u, 0u};
    }
  } else {
    char* base = ws + WS_V + (size_t)(j - 8) * 8 * HEAD_B + (size_t)(m0 + rt * 32) * 48;
    if (grp < 2) {
#pragma unroll
      for (int t = 0; t < 3; ++t) {
        if (t < ntile) {
          const f32x16& a = acc[t];
          const int n = 32 * (grp * 3 + t) + r, hl = (n * 205) >> 12, d = n - hl * 20;
          const unsigned off = (unsigned)hl * (unsigned)HEAD_B + h * 384 + d * 16;
#pragma unroll
          for (int sp = 0; sp < 2; ++sp) {
            u32x4 v = {cvtpk(a[8 * sp], a[8 * sp + 1]), cvtpk(a[8 * sp + 2], a[8 * sp + 3]),
                       cvtpk(a[8 * sp + 4], a[8 * sp + 5]), cvtpk(a[8 * sp + 6], a[8 * sp + 7])};
            *(u32x4*)(base + off + sp * 768) = v;
          }
        }
      }
    } else {
      const unsigned cv = (r & 3) == 0 ? 0x3F803F80u : 0u;
#pragma unroll
      for (int sp = 0; sp < 2; ++sp)
        *(u32x4*)(base + (size_t)(r >> 2) * HEAD_B + h * 384 + sp * 768 + (20 + (r & 3)) * 16) = (u32x4){cv, cv, cv, cv};
    }
  }
  if (my_extra) {
    const f32x16& e = acc[2];
    if (typ == 1) {
      if (h == 0) *(float*)(ws + WS_G + (size_t)(m0 + rt * 32 + r) * 4) = 1.0f / (1.0f + __expf(-e[0]));
    } else if (typ == 0) {
      char* base = ws + WS_K + (size_t)32 * HEAD_B + (size_t)(m0 + rt * 32) * 48 + r * 8;
#pragma unroll
      for (int g = 0; g < 3; ++g) {
        u32x2 v = {cvtpk(e[4 * g], e[4 * g + 1]), cvtpk(e[4 * g + 2], e[4 * g + 3])};
        if (g == 2 && h == 1) v = (u32x2){0x3F80u, 0u};
        *(u32x2*)(base + (2 * g + h) * 256) = v;
      }
    } else if (r < 24) {
      char* base = ws + WS_V + (size_t)32 * HEAD_B + (size_t)(m0 + rt * 32) * 48 + h * 384 + r * 16;
      const unsigned cv = r == 20 ? 0x3F803F80u : 0u;
#pragma unroll
      for (int sp = 0; sp < 2; ++sp) {
        u32x4 v = {cvtpk(e[8 * sp], e[8 * sp + 1]), cvtpk(e[8 * sp + 2], e[8 * sp + 3]),
                   cvtpk(e[8 * sp + 4], e[8 * sp + 5]), cvtpk(e[8 * sp + 6], e[8 * sp + 7])};
        if (r >= 20) v = (u32x4){cv, cv, cv, cv};
        *(u32x4*)(base + sp * 768) = v;
      }
    }
  }
  STAMP(3);
  STAMP_OUT(0, ws);
}

constexpr int A_CHUNK = 24576, A_KB = 12288, A_NSLOT = 3, A_LDS = 20 * 7 * 1024;

DI float half_max(float m) {
  auto rr = __builtin_amdgcn_permlane32_swap(__float_as_uint(m), __float_as_uint(m), false, false);
  return fmaxf(__uint_as_float(rr[0]), __uint_as_float(rr[1]));
}
DI float max16(const f32x16& s) {
  float a = fmaxf(fmaxf(s[0], s[1]), s[2]), b = fmaxf(fmaxf(s[3], s[4]), s[5]);
  a = fmaxf(fmaxf(a, s[6]), s[7]); b = fmaxf(fmaxf(b, s[8]), s[9]);
  a = fmaxf(fmaxf(a, s[10]), s[11]); b = fmaxf(fmaxf(b, s[12]), s[13]);
  a = fmaxf(fmaxf(a, s[14]), s[15]);
  return half_max(fmaxf(a, b));
}
DI f32x16 attn_safe(const char* kg, const char* vg, bf16x8 q1, bf16x4 q2, int r, int h) {
  if (h) q2 = (bf16x4){0, 0, 0, 0};
  f32x16 o = {};
  float m = -INFINITY;
#pragma unroll 1
  for (int t = 0; t < 64; ++t) {
    const char* kp = kg + t * 1536 + r * 8;
    const bf16x8 k1 = cat4(*(const bf16x4*)(kp + h * 512), *(const bf16x4*)(kp + h * 512 + 256));
    const bf16x4 k2 = *(const bf16x4*)(kp + 1024 + h * 256);
    const int vr = r < 24 ? r : 23;
    const bf16x8 v0 = *(const bf16x8*)(vg + t * 1536 + h * 384 + vr * 16);
    const bf16x8 v1 = *(const bf16x8*)(vg + t * 1536 + 768 + h * 384 + vr * 16);
    f32x16 s = {};
    s = MFMA32(k1, q1, s);
    s = MFMA8(k2, q2, s);
    const float mn = fmaxf(m, max16(s));
    const float al = __builtin_amdgcn_exp2f(m - mn);
    m = mn;
#pragma unroll
    for (int i = 0; i < 16; ++i) { o[i] *= al; s[i] = __builtin_amdgcn_exp2f(s[i] - mn); }
    u32x4 p0 = {cvtpk(s[0], s[1]), cvtpk(s[2], s[3]), cvtpk(s[4], s[5]), cvtpk(s[6], s[7])};
    u32x4 p1 = {cvtpk(s[8], s[9]), cvtpk(s[10], s[11]), cvtpk(s[12], s[13]), cvtpk(s[14], s[15])};
    o = MFMA32(v0, __builtin_bit_cast(bf16x8, p0), o);
    o = MFMA32(v1, __builtin_bit_cast(bf16x8, p1), o);
  }
  return o;
}
DI bool o_unsafe(const f32x16& o, int h) {
  float a = fabsf(o[0]) + fabsf(o[1]), b = fabsf(o[2]) + fabsf(o[3]);
#pragma unroll
  for (int i = 4; i < 8; i += 2) { a += fabsf(o[i]); b += fabsf(o[i + 1]); }
  float c = fabsf(o[8]) + fabsf(o[9]) + fabsf(o[10]) + fabsf(o[11]);
  if (h) c = (o[8] < 1.0e18f && o[8] > 1.0e-18f) ? 0.f : INFINITY;
  return !(a + b + c < 3.0e38f);
}
DI void attn_store(const f32x16& o, __amdgpu_buffer_rsrc_t og, int hh, int tok, float gfac, int h) {
  auto rr = __builtin_amdgcn_permlane32_swap(__float_as_uint(o[8]), __float_as_uint(o[8]), false, false);
  const float sc = gfac * __builtin_amdgcn_rcpf(__uint_as_float(rr[1]));
  u32x4 v = {cvtpk(o[0] * sc, o[1] * sc), cvtpk(o[2] * sc, o[3] * sc), cvtpk(o[4] * sc, o[5] * sc), cvtpk(o[6] * sc, o[7] * sc)};
  __builtin_amdgcn_raw_buffer_store_b128(v, og, ((hh * 2 + h) * S + tok) * 16, 0, 16);
  if (h == 0) {
    u32x2 t = {cvtpk(o[8] * sc, o[9] * sc), cvtpk(o[10] * sc, o[11] * sc)};
    __builtin_amdgcn_raw_buffer_store_b64(t, og, ((128 + (hh >> 1)) * S + tok) * 16 + (hh & 1) * 8, 0, 16);
  }
}
typedef __attribute__((address_space(1))) unsigned gu32;

__global__ __launch_bounds__(512, 1) void k2_attn(char* __restrict__ ws, u16* __restrict__ out) {
  __shared__ __attribute__((aligned(16))) char lds[A_LDS];
  STAMP_DECL STAMP(0);
  const int tid = threadIdx.x, lane = tid & 63, r = lane & 31, h = lane >> 5;
  const int wid = __builtin_amdgcn_readfirstlane(tid >> 6);
  const int xcd = blockIdx.x & 7, bi = blockIdx.x >> 3;
  const int hh = (bi >> 4) * 32 + xcd * 4 + ((bi >> 2) & 3), qb = bi & 3;
  const int kvh = hh < 32 ? hh : 32;
  const char* kg = ws + WS_K + (size_t)kvh * HEAD_B;
  const char* vg = ws + WS_V + (size_t)kvh * HEAD_B;
  const int tokA = qb * 512 + wid * 64 + r, tokB = tokA + 32;
  const unsigned lds0 = (unsigned)(uintptr_t)lds;
  const lds_cptr L = (lds_cptr)lds;

  const char* qg = ws + WS_Q + (size_t)hh * HEAD_B;
  const char* qp = qg + (tokA - r) * 48 + r * 8;
  const bf16x8 qA1 = cat4(*(const bf16x4*)(qp + h * 512), *(const bf16x4*)(qp + h * 512 + 256));
  const bf16x8 qB1 = cat4(*(const bf16x4*)(qp + 1536 + h * 512), *(const bf16x4*)(qp + 1536 + h * 512 + 256));
  bf16x4 qA2 = *(const bf16x4*)(qp + 1024), qB2 = *(const bf16x4*)(qp + 1536 + 1024);

  const float gA = *(const float*)(ws + WS_G + tokA * 4), gB = *(const float*)(ws + WS_G + tokB * 4);

  const char* dsrc = wid < 4 ? kg + wid * 3072 : vg + (wid - 4) * 3072;
  auto dma1 = [&](int c, int slot, int i) { glds16(dsrc + c * A_KB + i * 1024, lane * 16, lds0 + slot * A_CHUNK + (wid * 3 + i) * 1024); };
  auto dma = [&](int c, int slot) { dma1(c, slot, 0); dma1(c, slot, 1); dma1(c, slot, 2); };
  dma(0, 0);
  WAIT_BAR(0);
  dma(1, 1);
  const int mt = (xcd >> 1) * 8 + (bi & 7), nb = (xcd & 1) * 4 + (bi >> 3);
  const int c16 = lane & 15, g = lane >> 4;
  bf16x8 wf[5][5], of[5][4];
  const unsigned wvo = (g * D + c16) * 16;
  const char* wgs = ws + WS_W + ((size_t)wid * 20 * D + nb * 80) * 16;
#define WLOAD(c, n) do { if ((c) < 5) asm volatile("global_load_dwordx4 %0, %1, %2 offset:%c3" : "=v"(wf[(c) < 5 ? (c) : 0][n]) \
      : "v"(wvo), "s"(wgs + (c) * (4 * D * 16)), "i"((n) * 256) : "memory"); } while (0)

  const int kb1 = r * 8 + h * 512, kb2 = r * 8 + 1024 + h * 256, vb = A_KB + h * 384 + (r < 21 ? r : 21) * 16;
  int kb1b = kb1 + 256; PIN(kb1b);
  bf16x8 k1a = cat4(lds_b64(L + kb1), lds_b64(L + kb1b)), k1b;
  bf16x4 k2a = lds_b64(L + kb2), k2b;
  bf16x8 v0a = {}, v1a = {}, v0b = {}, v1b = {};
  const bf16x4 q2keepA = qA2, q2keepB = qB2;
  f32x16 sA, sB, oA = {}, oB = {};
  const f32x16 zero = {};
  {
    if (h) { qA2 = (bf16x4){0, 0, 0, 0}; qB2 = (bf16x4){0, 0, 0, 0}; }
    sA = MFMA32(k1a, qA1, zero); sB = MFMA32(k1a, qB1, zero);
    sA = MFMA8(k2a, qA2, sA);    sB = MFMA8(k2a, qB2, sB);
    const unsigned ma = cvtpk(-max16(sA), 0.f), mb = cvtpk(-max16(sB), 0.f);
    if (h) { qA2 = __builtin_bit_cast(bf16x4, (u32x2){ma, 0u}); qB2 = __builtin_bit_cast(bf16x4, (u32x2){mb, 0u}); }
    sA = MFMA32(k1a, qA1, zero);
    sA = MFMA8(k2a, qA2, sA);
  }
  u32x4 pA0 = {}, pA1 = {}, pB0 = {}, pB1 = {};

#define PRIO(n) __builtin_amdgcn_s_setprio(n)
#define EX(v) __builtin_amdgcn_exp2f(v)
#define GAP(MF, X, i, P, e) do { MF; SBAR(); X[i] = EX(X[i]); X[i + 1] = EX(X[i + 1]); X[i + 2] = EX(X[i + 2]); X[i + 3] = EX(X[i + 3]); \
    P[e] = cvtpk(X[i], X[i + 1]); P[e + 1] = cvtpk(X[i + 2], X[i + 3]); PIN(P); SBAR(); } while (0)
#define BF(p) __builtin_bit_cast(bf16x8, p)
#define PHASE(X, PX0, PX1, Y, OY, PY0, PY1, QY1, QY2, K1, K2, V0, V1) do { \
    GAP(Y = MFMA32(K1, QY1, zero), X, 0, PX0, 0);  \
    GAP(Y = MFMA8(K2, QY2, Y),     X, 4, PX0, 2);  \
    GAP(OY = MFMA32(V0, BF(PY0), OY), X, 8, PX1, 0);  \
    GAP(OY = MFMA32(V1, BF(PY1), OY), X, 12, PX1, 2); } while (0)
#define TILE(KP, VP, K1C, K2C, V0C, V1C, K1N, K2N, V0N, V1N) do { \
    K1N = cat4(lds_b64((KP) + kb1), lds_b64((KP) + kb1b)); K2N = lds_b64((KP) + kb2); V0N = lds_b128((VP) + vb); V1N = lds_b128((VP) + vb + 768); SBAR(); \
    PHASE(sA, pA0, pA1, sB, oB, pB0, pB1, qB1, qB2, K1C, K2C, V0C, V1C); \
    __builtin_amdgcn_s_waitcnt(0xC07F); SBAR(); \
    PHASE(sB, pB0, pB1, sA, oA, pA0, pA1, qA1, qA2, K1N, K2N, V0N, V1N); } while (0)

  STAMP(1);
#pragma unroll
  for (int c = 0; c < 8; ++c) {
    const int slot = c % A_NSLOT, nslot = (c + 1) % A_NSLOT;
    const lds_cptr cp = L + slot * A_CHUNK, np = L + nslot * A_CHUNK;
    const bool mid = c >= 1 && c < 7;
    PRIO(3);
    if (mid) dma1(c + 1, nslot, 1);
    TILE(cp + 1 * 1536, cp + 0 * 1536, k1a, k2a, v0a, v1a, k1b, k2b, v0b, v1b);
    if (mid) dma1(c + 1, nslot, 2);
    TILE(cp + 2 * 1536, cp + 1 * 1536, k1b, k2b, v0b, v1b, k1a, k2a, v0a, v1a);
    PRIO(2);
    WLOAD(c, 0);
    TILE(cp + 3 * 1536, cp + 2 * 1536, k1a, k2a, v0a, v1a, k1b, k2b, v0b, v1b);
    WLOAD(c, 1);
    TILE(cp + 4 * 1536, cp + 3 * 1536, k1b, k2b, v0b, v1b, k1a, k2a, v0a, v1a);
    PRIO(1);
    WLOAD(c, 2);
    TILE(cp + 5 * 1536, cp + 4 * 1536, k1a, k2a, v0a, v1a, k1b, k2b, v0b, v1b);
    WLOAD(c, 3);
    TILE(cp + 6 * 1536, cp + 5 * 1536, k1b, k2b, v0b, v1b, k1a, k2a, v0a, v1a);
    PRIO(0);
    WLOAD(c, 4);
    TILE(cp + 7 * 1536, cp + 6 * 1536, k1a, k2a, v0a, v1a, k1b, k2b, v0b, v1b);
    if (c < 7) WAIT_BAR(0);
    PRIO(3);
    if (c + 2 < 8) dma1(c + 2, (c + 2) % A_NSLOT, 0);
    TILE(np, cp + 7 * 1536, k1b, k2b, v0b, v1b, k1a, k2a, v0a, v1a);
  }
  STAMP(2);
  oB = MFMA32(v0a, BF(pB0), oB);
  oB = MFMA32(v1a, BF(pB1), oB);

  if (__builtin_expect(__any(o_unsafe(oA, h) || o_unsafe(oB, h)), 0)) {
    oA = attn_safe(kg, vg, qA1, q2keepA, r, h);
    oB = attn_safe(kg, vg, qB1, q2keepB, r, h);
  }
  const __amdgpu_buffer_rsrc_t og = __builtin_amdgcn_make_buffer_rsrc(ws + WS_O, 0, 160 * S * 16, 0x00020000);
  attn_store(oA, og, hh, tokA, hh < 32 ? gA : 1.0f - gA, h);
  attn_store(oB, og, hh, tokB, hh < 32 ? gB : 1.0f - gB, h);
  SBAR();

  asm volatile("s_waitcnt vmcnt(0) lgkmcnt(0)\n\ts_barrier" ::: "memory");
  STAMP(3);
  gu32* flags = (gu32*)(ws + WS_F);
  if (wid == 0) {
    if (lane < 8) __hip_atomic_store(flags + (qb * 8 + lane) * 64 + hh, 1u, __ATOMIC_RELAXED, __HIP_MEMORY_SCOPE_AGENT);
    gu32* pf = flags + ((mt >> 3) * 8 + nb) * 64 + lane;
    for (unsigned spins = 0; spins < (1u << 22); ++spins) {
      if (__all(__hip_atomic_load(pf, __ATOMIC_RELAXED, __HIP_MEMORY_SCOPE_AGENT) == 1u)) break;
      __builtin_amdgcn_s_sleep(1);
    }
  }
  asm volatile("s_barrier" ::: "memory");
  STAMP(4);
  const int ob = ((wid * 20 + g) * S + mt * 64 + c16) * 16;
  auto oload = [&](int s) {
#pragma unroll
    for (int m = 0; m < 4; ++m)
      of[s][m] = __builtin_bit_cast(bf16x8, __builtin_amdgcn_raw_buffer_load_b128(og, ob, s * 4 * S * 16 + ((m + wid) & 3) * 256, 16));
  };
#pragma unroll
  for (int s = 0; s < 4; ++s) oload(s);
  SBAR();
  f32x4 acc[4][5] = {};
#pragma unroll
  for (int s = 0; s < 5; ++s) {
#pragma unroll
    for (int m = 3; m >= 0; --m)
#pragma unroll
      for (int n = 0; n < 5; ++n) acc[m][n] = MFMA16(wf[s][n], of[s][m], acc[m][n]);
    SBAR();
    if (s == 0) oload(4);
    SBAR();
  }
  const int nh = wid >> 2;
#pragma unroll
  for (int m = 3; m >= 0; --m)
#pragma unroll
    for (int n = 0; n < 5; ++n) {
      const int gm = (m + wid) & 3, owner = gm + (n >= 3 ? 4 : 0), sl = (wid - owner) & 7;
      if (m != 0 || (n >= 3) != (nh == 1)) *(f32x4*)(lds + ((gm * 5 + n) * 7 + sl - 1) * 1024 + lane * 16) = acc[m][n];
    }
  __syncthreads();
  u16* op = out + (size_t)(mt * 64 + (wid & 3) * 16 + c16) * D + nb * 80 + g * 4;
#pragma unroll
  for (int n = 0; n < 5; ++n) {
    if ((n >= 3) == (nh == 1)) {
      f32x4 a = acc[0][n];
#pragma unroll
      for (int sl = 0; sl < 7; ++sl) a += *(const f32x4*)(lds + (((wid & 3) * 5 + n) * 7 + sl) * 1024 + lane * 16);
      *(u32x2*)(op + n * 16) = (u32x2){cvtpk(a[0], a[1]), cvtpk(a[2], a[3])};
    }
  }
  STAMP_OUT(1, ws);
}

extern "C" void kernel_launch(void* const* d_in, const int* in_sizes, int n_in, void* d_out, int out_size, void* d_ws, size_t ws_size,
                              hipStream_t stream) {
  const u16* x = (const u16*)d_in[0];
  const u16* wqkv = (const u16*)d_in[1];
  const u16* woa = (const u16*)d_in[2];
  const u16* wq = (const u16*)d_in[4];
  const u16* wk = (const u16*)d_in[5];
  const u16* wv = (const u16*)d_in[6];
  const u16* wob = (const u16*)d_in[7];
  const u16* gw = (const u16*)d_in[8];
  char* ws = (char*)d_ws;
  k1_proj<<<256, 768, 0, stream>>>(x, wqkv, wq, wk, wv, gw, woa, wob, ws, (const u16*)d_out);
  k2_attn<<<256, 512, 0, stream>>>(ws, (u16*)d_out);
}
```

```cpp
#include <hip/hip_runtime.h>
#include <hip/hip_bf16.h>
#include <stdint.h>
#include <type_traits>

typedef __attribute__((ext_vector_type(8))) short bf16x8;
typedef __attribute__((ext_vector_type(4))) short bf16x4;
typedef __attribute__((ext_vector_type(16))) float f32x16;
typedef __attribute__((ext_vector_type(4))) float f32x4;
typedef __attribute__((ext_vector_type(2))) float f32x2;
typedef __attribute__((ext_vector_type(4))) unsigned u32x4;
typedef __attribute__((ext_vector_type(2))) unsigned u32x2;
typedef __attribute__((ext_vector_type(2))) __bf16 bf16x2_t;
typedef __attribute__((address_space(3))) const char* lds_cptr;
typedef unsigned short u16;

#define DI __device__ __forceinline__
#define SBAR() __builtin_amdgcn_sched_barrier(0)
#define PIN(x) asm volatile("" : "+v"(x))
#define MFMA32(a, b, c) __builtin_amdgcn_mfma_f32_32x32x16_bf16((a), (b), (c), 0, 0, 0)
#define MFMA8(a, b, c) __builtin_amdgcn_mfma_f32_32x32x8bf16_1k((a), (b), (c), 0, 0, 0)
#define MFMA16(a, b, c) __builtin_amdgcn_mfma_f32_16x16x32_bf16((a), (b), (c), 0, 0, 0)

#define STAMP_DECL
#define STAMP(i)
#define STAMP_OUT(kid, wsb)

constexpr int S = 2048, D = 640;
constexpr float QSCALE = 0.22360679774997896f * 1.4426950408889634f;

constexpr size_t HEAD_B = (size_t)S * 48;
constexpr size_t WS_Q = 0;
constexpr size_t WS_K = WS_Q + 64 * HEAD_B;
constexpr size_t WS_V = WS_K + 33 * HEAD_B;
constexpr size_t WS_G = WS_V + 33 * HEAD_B;
constexpr size_t WS_O = WS_G + S * 4;
constexpr size_t WS_W = WS_O + (size_t)160 * S * 16;
constexpr size_t WS_F = WS_W + (size_t)160 * D * 16;
constexpr size_t WS_END = WS_F + 8192;

DI unsigned cvtpk(float lo, float hi) {
  f32x2 v = {lo, hi};
  return __builtin_bit_cast(unsigned, __builtin_convertvector(v, bf16x2_t));
}
DI void glds16(const void* sbase, unsigned voff, unsigned lds_dst) {
  unsigned keep;
  asm volatile("s_mov_b32 %0, m0\n\ts_mov_b32 m0, %3\n\ts_nop 0\n\tglobal_load_lds_dwordx4 %1, %2\n\ts_mov_b32 m0, %0"
               : "=&s"(keep) : "v"(voff), "s"(sbase), "s"(lds_dst) : "memory");
}
DI bf16x8 lds_b128(lds_cptr p) { return *(const __attribute__((address_space(3))) bf16x8*)p; }
DI bf16x4 lds_b64(lds_cptr p) { return *(const __attribute__((address_space(3))) bf16x4*)p; }
DI bf16x4 lds_tr(lds_cptr p) {
  return __builtin_amdgcn_ds_read_tr16_b64_v4i16((__attribute__((address_space(3))) bf16x4*)p);
}
DI bf16x8 cat4(bf16x4 a, bf16x4 b) { return __builtin_shufflevector(a, b, 0, 1, 2, 3, 4, 5, 6, 7); }
#define WAIT_BAR(N) asm volatile("s_waitcnt vmcnt(" #N ") lgkmcnt(0)\n\ts_barrier" ::: "memory")

constexpr int P_XB = 16384, P_WB = 20480, P_EB = 3072, P_STAGE = P_XB + P_WB + P_EB, P_NST = 4;
constexpr int P_GATE = P_STAGE * P_NST, P_LDS = P_GATE + 2048;

__global__ __launch_bounds__(768, 1) void k1_proj(const u16* __restrict__ x, const u16* __restrict__ wqkv, const u16* __restrict__ wq,
                                                  const u16* __restrict__ wk, const u16* __restrict__ wv, const u16* __restrict__ gw,
                                                  const u16* __restrict__ woa, const u16* __restrict__ wob, char* __restrict__ ws) {
  __shared__ __attribute__((aligned(16))) char lds[P_LDS];
  STAMP_DECL STAMP(0);
  const int tid = threadIdx.x, lane = tid & 63, r = lane & 31, h = lane >> 5;
  const int wid = __builtin_amdgcn_readfirstlane(tid >> 6);
  const int xcd = blockIdx.x & 7, bi = blockIdx.x >> 3;
  const int mt = (xcd & 1) * 8 + (bi & 7), j = (xcd >> 1) * 4 + (bi >> 3);
  const int m0 = mt * 128;
  const int typ = j >> 2;
  const bool wg_extra = typ == 0 || typ == 3;
  const int ex_rt = j & 3, ex_typ = typ == 3;
  const unsigned lds0 = (unsigned)(uintptr_t)lds;
  const lds_cptr L = (lds_cptr)lds;
  const bool vtype = typ == 2;
  const int rt = wid & 3, grp = wid >> 2;
  const bool my_extra = !vtype && grp == 1 && rt == ex_rt;
  f32x16 acc[3] = {};

  if (grp == 2) {
    unsigned xoff[2], woff[5];
#pragma unroll
    for (int i = 0; i < 2; ++i) {
      const int row = rt * 32 + i * 8 + (lane >> 3), c = (lane & 7) ^ ((row >> 1) & 7);
      xoff[i] = (unsigned)(m0 + row) * 1280u + c * 16;
    }
    auto stage_x = [&](int s) {
#pragma unroll
      for (int i = 0; i < 4; ++i)
        glds16((const char*)x + s * 128 + (i >> 1) * 16 * 1280, xoff[i & 1], lds0 + (s & 3) * P_STAGE + (rt * 4 + i) * 1024);
    };
    stage_x(0);
    const u16* wsrc = (typ < 3) ? (wqkv + j * 160) : (wq + (j - 12) * 160);
    const unsigned ldw = (typ < 3) ? 3840u : 1280u;
    const u16* esrc = ex_typ ? wv : wk;
#pragma unroll
    for (int i = 0; i < 5; ++i) {
      const int idx = i * 64 + lane, row = (idx * 205) >> 12, gr = idx - row * 20;
      woff[i] = (unsigned)(rt * 16 + row) * ldw + gr * 16;
    }
    auto stage_w = [&](int s) {
      const unsigned dst = lds0 + (s & 3) * P_STAGE + P_XB;
      const char* wb = (const char*)wsrc + (size_t)s * 64 * ldw;
#pragma unroll
      for (int i = 0; i < 5; ++i) glds16(wb, woff[i], dst + (rt * 5 + i) * 1024);
      if (wg_extra && rt < 3) {
        if (rt < 2 || lane < 32) glds16((const char*)esrc + s * 2560 + rt * 1024, lane * 16, dst + P_WB + rt * 1024);
      }
    };
    auto stage = [&](int s) { stage_x(s); stage_w(s); };
    stage_w(0);
    if (typ == 1 && rt == 3) {
      glds16(gw, lane * 16, lds0 + P_GATE);
      if (lane < 16) glds16((const char*)gw + 1024, lane * 16, lds0 + P_GATE + 1024);
    }
    stage(1);
#pragma unroll 1
    for (int s = 0; s < 10; ++s) {
      if (s < 9) WAIT_BAR(9); else WAIT_BAR(0);
      if (s + 2 < 10) stage(s + 2);
    }
  } else {
    if (tid < 400) {
      const int task = blockIdx.x * 400 + tid;
      u16 wv8[8];
      const int gr = task / 640, n = task - gr * 640;
      const bool mainp = gr < 128;
      const int hd0 = mainp ? (gr >> 1) : 2 * (gr - 128);
      const u16* w = (hd0 < 32 ? woa + hd0 * 20 * 640 : wob + (hd0 - 32) * 20 * 640) + (mainp ? 4 * (gr & 1) : 16) * 640 + n;
      const int q2 = mainp ? 8 * 640 : 20 * 640;
#pragma unroll
      for (int jj = 0; jj < 4; ++jj) { wv8[jj] = w[jj * 640]; wv8[4 + jj] = w[q2 + jj * 640]; }
      *(u32x4*)(ws + WS_W + (size_t)task * 16) = (u32x4){wv8[0] | ((unsigned)wv8[1] << 16), wv8[2] | ((unsigned)wv8[3] << 16),
                                                         wv8[4] | ((unsigned)wv8[5] << 16), wv8[6] | ((unsigned)wv8[7] << 16)};
    }
    if (wid == 7 && blockIdx.x < 8) *(u32x4*)(ws + WS_F + blockIdx.x * 1024 + lane * 16) = (u32x4){0u, 0u, 0u, 0u};
    const int xsw = h ^ ((r >> 1) & 7), xa = (rt * 32 + r) * 128;
    const int q4 = (lane & 15) >> 2, p4 = lane & 3, b16 = (lane >> 4) & 1;
    const int wbase = P_XB + (8 * h + q4) * 320 + (16 * b16 + 4 * p4) * 2 + grp * 192;
    const int ebase = P_XB + P_WB + (8 * h + q4) * 40 + (16 * b16 + 4 * p4) * 2;
    auto mainloop = [&](auto vt_, auto mode_) {
      constexpr bool VT = decltype(vt_)::value;
      constexpr int MODE = decltype(mode_)::value;
      struct Frag { bf16x8 x, w0, w1, w2; };
      auto rd = [&](Frag& f, int s, int ks) {
        const lds_cptr st = L + (s & 3) * P_STAGE;
        const lds_cptr wp = st + wbase + ks * 16 * 320;
        f.x = lds_b128(st + xa + ((2 * ks) ^ xsw) * 16);
        f.w0 = cat4(lds_tr(wp), lds_tr(wp + 4 * 320));
        f.w1 = cat4(lds_tr(wp + 64), lds_tr(wp + 64 + 4 * 320));
        if constexpr (MODE == 0) f.w2 = cat4(lds_tr(wp + 128), lds_tr(wp + 128 + 4 * 320));
        if constexpr (MODE == 2 || MODE == 3) {
          const lds_cptr ep = st + ebase + ks * 16 * 40;
          f.w2 = cat4(lds_tr(ep), lds_tr(ep + 4 * 40));
        }
        if constexpr (MODE == 4) f.w2 = lds_b128(L + P_GATE + (s * 64 + ks * 16 + 8 * h) * 2);
      };
      auto mm = [&](f32x16& a, const bf16x8& xf, const bf16x8& wf) {
        if constexpr (VT) a = MFMA32(xf, wf, a); else a = MFMA32(wf, xf, a);
      };
      auto mmA = [&](const Frag& f) {
        mm(acc[0], f.x, f.w0); mm(acc[1], f.x, f.w1);
        if constexpr (MODE == 0) mm(acc[2], f.x, f.w2);
        if constexpr (MODE == 2 || MODE == 4) acc[2] = MFMA32(f.w2, f.x, acc[2]);
        if constexpr (MODE == 3) acc[2] = MFMA32(f.x, f.w2, acc[2]);
      };
      auto ilv = [&]() {
#pragma unroll
        for (int i = 0; i < (MODE == 1 ? 2 : 3); ++i) {
          __builtin_amdgcn_sched_group_barrier(0x008, 1, 0);
          __builtin_amdgcn_sched_group_barrier(0x100, 3, 0);
        }
      };
      Frag f0, f1;
      asm volatile("s_barrier" ::: "memory");
      rd(f0, 0, 0);
      SBAR();
#pragma unroll 1
      for (int s = 0; s < 9; ++s) {
        rd(f1, s, 1); mmA(f0); ilv(); SBAR();
        rd(f0, s, 2); mmA(f1); ilv(); SBAR();
        rd(f1, s, 3); mmA(f0); ilv(); SBAR();
        asm volatile("s_waitcnt lgkmcnt(0)\n\ts_barrier" ::: "memory");
        rd(f0, s + 1, 0); mmA(f1); ilv(); SBAR();
      }
      rd(f1, 9, 1); mmA(f0); ilv(); SBAR();
      rd(f0, 9, 2); mmA(f1); ilv(); SBAR();
      rd(f1, 9, 3); mmA(f0); ilv(); SBAR();
      mmA(f1);
    };
    using std::integral_constant;
    if (vtype) {
      if (grp == 0) mainloop(integral_constant<bool, true>{}, integral_constant<int, 0>{});
      else          mainloop(integral_constant<bool, true>{}, integral_constant<int, 1>{});
    } else if (grp == 0) mainloop(integral_constant<bool, false>{}, integral_constant<int, 0>{});
    else if (!my_extra)  mainloop(integral_constant<bool, false>{}, integral_constant<int, 1>{});
    else if (typ == 0)   mainloop(integral_constant<bool, false>{}, integral_constant<int, 2>{});
    else if (typ == 3)   mainloop(integral_constant<bool, false>{}, integral_constant<int, 3>{});
    else                 mainloop(integral_constant<bool, false>{}, integral_constant<int, 4>{});
  }
  STAMP(2);

  const int ntile = grp == 0 ? 3 : 2;
  if (!vtype) {
    char* base = ws + (typ == 1 ? WS_K + (size_t)(j - 4) * 8 * HEAD_B : WS_Q + (size_t)(typ == 0 ? j : j - 8) * 8 * HEAD_B) + (size_t)(m0 + rt * 32) * 48;
    if (grp < 2) {
      const f32x2 sc = {typ == 1 ? 1.0f : QSCALE, typ == 1 ? 1.0f : QSCALE};
#pragma unroll
      for (int t = 0; t < 3; ++t) {
        if (t < ntile) {
          const f32x16& a = acc[t];
#pragma unroll
          for (int g = 0; g < 4; ++g) {
            const int qd = 8 * (grp * 3 + t) + 2 * g + h, hl = (qd * 205) >> 10, dq = qd - hl * 5;
            const unsigned off = (unsigned)hl * (unsigned)HEAD_B + dq * 256 + r * 8;
            const f32x2 lo = (f32x2){a[4 * g], a[4 * g + 1]} * sc, hi = (f32x2){a[4 * g + 2], a[4 * g + 3]} * sc;
            *(u32x2*)(base + off) = (u32x2){cvtpk(lo[0], lo[1]), cvtpk(hi[0], hi[1])};
          }
        }
      }
    } else if (typ == 1) {
#pragma unroll
      for (int i = 0; i < 4; ++i) *(u32x2*)(base + (size_t)(2 * i + h) * HEAD_B + 5 * 256 + r * 8) = (u32x2){0x3F80u, 0u};
    }
  } else {
    char* base = ws + WS_V + (size_t)(j - 8) * 8 * HEAD_B + (size_t)(m0 + rt * 32) * 48;
    if (grp < 2) {
#pragma unroll
      for (int t = 0; t < 3; ++t) {
        if (t < ntile) {
          const f32x16& a = acc[t];
          const int n = 32 * (grp * 3 + t) + r, hl = (n * 205) >> 12, d = n - hl * 20;
          const unsigned off = (unsigned)hl * (unsigned)HEAD_B + h * 384 + d * 16;
#pragma unroll
          for (int sp = 0; sp < 2; ++sp) {
            u32x4 v = {cvtpk(a[8 * sp], a[8 * sp + 1]), cvtpk(a[8 * sp + 2], a[8 * sp + 3]),
                       cvtpk(a[8 * sp + 4], a[8 * sp + 5]), cvtpk(a[8 * sp + 6], a[8 * sp + 7])};
            *(u32x4*)(base + off + sp * 768) = v;
          }
        }
      }
    } else {
      const unsigned cv = (r & 3) == 0 ? 0x3F803F80u : 0u;
#pragma unroll
      for (int sp = 0; sp < 2; ++sp)
        *(u32x4*)(base + (size_t)(r >> 2) * HEAD_B + h * 384 + sp * 768 + (20 + (r & 3)) * 16) = (u32x4){cv, cv, cv, cv};
    }
  }
  if (my_extra) {
    const f32x16& e = acc[2];
    if (typ == 1) {
      if (h == 0) *(float*)(ws + WS_G + (size_t)(m0 + rt * 32 + r) * 4) = 1.0f / (1.0f + __expf(-e[0]));
    } else if (typ == 0) {
      char* base = ws + WS_K + (size_t)32 * HEAD_B + (size_t)(m0 + rt * 32) * 48 + r * 8;
#pragma unroll
      for (int g = 0; g < 3; ++g) {
        u32x2 v = {cvtpk(e[4 * g], e[4 * g + 1]), cvtpk(e[4 * g + 2], e[4 * g + 3])};
        if (g == 2 && h == 1) v = (u32x2){0x3F80u, 0u};
        *(u32x2*)(base + (2 * g + h) * 256) = v;
      }
    } else if (r < 24) {
      char* base = ws + WS_V + (size_t)32 * HEAD_B + (size_t)(m0 + rt * 32) * 48 + h * 384 + r * 16;
      const unsigned cv = r == 20 ? 0x3F803F80u : 0u;
#pragma unroll
      for (int sp = 0; sp < 2; ++sp) {
        u32x4 v = {cvtpk(e[8 * sp], e[8 * sp + 1]), cvtpk(e[8 * sp + 2], e[8 * sp + 3]),
                   cvtpk(e[8 * sp + 4], e[8 * sp + 5]), cvtpk(e[8 * sp + 6], e[8 * sp + 7])};
        if (r >= 20) v = (u32x4){cv, cv, cv, cv};
        *(u32x4*)(base + sp * 768) = v;
      }
    }
  }
  STAMP(3);
  STAMP_OUT(0, ws);
}

constexpr int A_CHUNK = 24576, A_KB = 12288, A_NSLOT = 3, A_LDS = 20 * 7 * 1024;

DI float half_max(float m) {
  auto rr = __builtin_amdgcn_permlane32_swap(__float_as_uint(m), __float_as_uint(m), false, false);
  return fmaxf(__uint_as_float(rr[0]), __uint_as_float(rr[1]));
}
DI float max16(const f32x16& s) {
  float a = fmaxf(fmaxf(s[0], s[1]), s[2]), b = fmaxf(fmaxf(s[3], s[4]), s[5]);
  a = fmaxf(fmaxf(a, s[6]), s[7]); b = fmaxf(fmaxf(b, s[8]), s[9]);
  a = fmaxf(fmaxf(a, s[10]), s[11]); b = fmaxf(fmaxf(b, s[12]), s[13]);
  a = fmaxf(fmaxf(a, s[14]), s[15]);
  return half_max(fmaxf(a, b));
}
DI f32x16 attn_safe(const char* kg, const char* vg, bf16x8 q1, bf16x4 q2, int r, int h) {
  if (h) q2 = (bf16x4){0, 0, 0, 0};
  f32x16 o = {};
  float m = -INFINITY;
#pragma unroll 1
  for (int t = 0; t < 64; ++t) {
    const char* kp = kg + t * 1536 + r * 8;
    const bf16x8 k1 = cat4(*(const bf16x4*)(kp + h * 512), *(const bf16x4*)(kp + h * 512 + 256));
    const bf16x4 k2 = *(const bf16x4*)(kp + 1024 + h * 256);
    const int vr = r < 24 ? r : 23;
    const bf16x8 v0 = *(const bf16x8*)(vg + t * 1536 + h * 384 + vr * 16);
    const bf16x8 v1 = *(const bf16x8*)(vg + t * 1536 + 768 + h * 384 + vr * 16);
    f32x16 s = {};
    s = MFMA32(k1, q1, s);
    s = MFMA8(k2, q2, s);
    const float mn = fmaxf(m, max16(s));
    const float al = __builtin_amdgcn_exp2f(m - mn);
    m = mn;
#pragma unroll
    for (int i = 0; i < 16; ++i) { o[i] *= al; s[i] = __builtin_amdgcn_exp2f(s[i] - mn); }
    u32x4 p0 = {cvtpk(s[0], s[1]), cvtpk(s[2], s[3]), cvtpk(s[4], s[5]), cvtpk(s[6], s[7])};
    u32x4 p1 = {cvtpk(s[8], s[9]), cvtpk(s[10], s[11]), cvtpk(s[12], s[13]), cvtpk(s[14], s[15])};
    o = MFMA32(v0, __builtin_bit_cast(bf16x8, p0), o);
    o = MFMA32(v1, __builtin_bit_cast(bf16x8, p1), o);
  }
  return o;
}
DI bool o_unsafe(const f32x16& o, int h) {
  float a = fabsf(o[0]) + fabsf(o[1]), b = fabsf(o[2]) + fabsf(o[3]);
#pragma unroll
  for (int i = 4; i < 8; i += 2) { a += fabsf(o[i]); b += fabsf(o[i + 1]); }
  float c = fabsf(o[8]) + fabsf(o[9]) + fabsf(o[10]) + fabsf(o[11]);
  if (h) c = (o[8] < 1.0e36f && o[8] > 1.0e-25f) ? 0.f : INFINITY;
  return !(a + b + c < 3.0e38f);
}
DI void attn_store(const f32x16& o, __amdgpu_buffer_rsrc_t og, int hh, int tok, float gfac, int h) {
  auto rr = __builtin_amdgcn_permlane32_swap(__float_as_uint(o[8]), __float_as_uint(o[8]), false, false);
  const float sc = gfac * __builtin_amdgcn_rcpf(__uint_as_float(rr[1]));
  u32x4 v = {cvtpk(o[0] * sc, o[1] * sc), cvtpk(o[2] * sc, o[3] * sc), cvtpk(o[4] * sc, o[5] * sc), cvtpk(o[6] * sc, o[7] * sc)};
  __builtin_amdgcn_raw_buffer_store_b128(v, og, ((hh * 2 + h) * S + tok) * 16, 0, 16);
  if (h == 0) {
    u32x2 t = {cvtpk(o[8] * sc, o[9] * sc), cvtpk(o[10] * sc, o[11] * sc)};
    __builtin_amdgcn_raw_buffer_store_b64(t, og, ((128 + (hh >> 1)) * S + tok) * 16 + (hh & 1) * 8, 0, 16);
  }
}
typedef __attribute__((address_space(1))) unsigned gu32;

__global__ __launch_bounds__(512, 1) void k2_attn(char* __restrict__ ws, u16* __restrict__ out) {
  __shared__ __attribute__((aligned(16))) char lds[A_LDS];
  STAMP_DECL STAMP(0);
  const int tid = threadIdx.x, lane = tid & 63, r = lane & 31, h = lane >> 5;
  const int wid = __builtin_amdgcn_readfirstlane(tid >> 6);
  const int xcd = blockIdx.x & 7, bi = blockIdx.x >> 3;
  const int hh = (bi >> 4) * 32 + xcd * 4 + ((bi >> 2) & 3), qb = bi & 3;
  const int kvh = hh < 32 ? hh : 32;
  const char* kg = ws + WS_K + (size_t)kvh * HEAD_B;
  const char* vg = ws + WS_V + (size_t)kvh * HEAD_B;
  const int tokA = qb * 512 + wid * 64 + r, tokB = tokA + 32;
  const unsigned lds0 = (unsigned)(uintptr_t)lds;
  const lds_cptr L = (lds_cptr)lds;

  const int kb1 = r * 8 + h * 512, kb2 = r * 8 + 1024 + h * 256, vb = A_KB + h * 384 + (r < 21 ? r : 21) * 16;
  bf16x4 qa0, qa1, qb0, qb1, qA2, qB2, ka0, ka1, k2a, k2b;
  {
    const char* qs = ws + WS_Q + (size_t)hh * HEAD_B + (size_t)(qb * 512 + wid * 64) * 48;
    asm volatile("global_load_dwordx2 %0, %9, %12\n\tglobal_load_dwordx2 %1, %9, %12 offset:256\n\tglobal_load_dwordx2 %2, %10, %12\n\t"
                 "global_load_dwordx2 %3, %9, %13\n\tglobal_load_dwordx2 %4, %9, %13 offset:256\n\tglobal_load_dwordx2 %5, %11, %13 offset:1024\n\t"
                 "global_load_dwordx2 %6, %9, %13 offset:1536\n\tglobal_load_dwordx2 %7, %9, %13 offset:1792\n\tglobal_load_dwordx2 %8, %11, %13 offset:2560"
                 : "=&v"(ka0), "=&v"(ka1), "=&v"(k2a), "=&v"(qa0), "=&v"(qa1), "=&v"(qA2), "=&v"(qb0), "=&v"(qb1), "=&v"(qB2)
                 : "v"(kb1), "v"(kb2), "v"(r * 8), "s"(kg), "s"(qs) : "memory");
  }

  const char* dsrc = wid < 4 ? kg + wid * 3072 : vg + (wid - 4) * 3072;
  auto dma1 = [&](int c, int slot, int i) { glds16(dsrc + c * A_KB + i * 1024, lane * 16, lds0 + slot * A_CHUNK + (wid * 3 + i) * 1024); };
  auto dma = [&](int c, int slot) { dma1(c, slot, 0); dma1(c, slot, 1); dma1(c, slot, 2); };
  dma(0, 0);
  asm volatile("s_waitcnt vmcnt(3)" : "+v"(ka0), "+v"(ka1), "+v"(k2a), "+v"(qa0), "+v"(qa1), "+v"(qA2), "+v"(qb0), "+v"(qb1), "+v"(qB2) :: "memory");
  const bf16x8 qA1 = cat4(qa0, qa1), qB1 = cat4(qb0, qb1);
  const int mt = (xcd >> 1) * 8 + (bi & 7), nb = (xcd & 1) * 4 + (bi >> 3);
  const int c16 = lane & 15, g = lane >> 4;
  bf16x8 wf[5][5], of[5][4];
  const unsigned wvo = (g * D + c16) * 16;
  const char* wgs = ws + WS_W + ((size_t)wid * 20 * D + nb * 80) * 16;
#define WLOAD(c, n) do { if ((c) < 5) asm volatile("global_load_dwordx4 %0, %1, %2 offset:%c3" : "=v"(wf[(c) < 5 ? (c) : 0][n]) \
      : "v"(wvo), "s"(wgs + (c) * (4 * D * 16)), "i"((n) * 256) : "memory"); } while (0)

  int kb1b = kb1 + 256; PIN(kb1b);
  bf16x8 k1a = cat4(ka0, ka1), k1b;
  bf16x8 v0a = {}, v1a = {}, v0b = {}, v1b = {};
  const bf16x4 q2keepA = qA2, q2keepB = qB2;
  f32x16 sA, sB, oA = {}, oB = {};
  const f32x16 zero = {};
  {
    if (h) { qA2 = (bf16x4){0, 0, 0, 0}; qB2 = (bf16x4){0, 0, 0, 0}; }
    sA = MFMA32(k1a, qA1, zero); sB = MFMA32(k1a, qB1, zero);
    sA = MFMA8(k2a, qA2, sA);    sB = MFMA8(k2a, qB2, sB);
    const unsigned ma = cvtpk(-max16(sA), 0.f), mb = cvtpk(-max16(sB), 0.f);
    if (h) { qA2 = __builtin_bit_cast(bf16x4, (u32x2){ma, 0u}); qB2 = __builtin_bit_cast(bf16x4, (u32x2){mb, 0u}); }
    sA = MFMA32(k1a, qA1, zero);
    sA = MFMA8(k2a, qA2, sA);
  }
  u32x4 pA0 = {}, pA1 = {}, pB0 = {}, pB1 = {};
  SBAR();
  WAIT_BAR(0);
  dma(1, 1);
  const float gA = *(const float*)(ws + WS_G + tokA * 4), gB = *(const float*)(ws + WS_G + tokB * 4);

#define PRIO(n) __builtin_amdgcn_s_setprio(n)
#define EX(v) __builtin_amdgcn_exp2f(v)
#define GAP(MF, X, i, P, e) do { MF; SBAR(); X[i] = EX(X[i]); X[i + 1] = EX(X[i + 1]); X[i + 2] = EX(X[i + 2]); X[i + 3] = EX(X[i + 3]); \
    P[e] = cvtpk(X[i], X[i + 1]); P[e + 1] = cvtpk(X[i + 2], X[i + 3]); PIN(P); SBAR(); } while (0)
#define BF(p) __builtin_bit_cast(bf16x8, p)
#define PHASE(X, PX0, PX1, Y, OY, PY0, PY1, QY1, QY2, K1, K2, V0, V1) do { \
    GAP(Y = MFMA32(K1, QY1, zero), X, 0, PX0, 0);  \
    GAP(Y = MFMA8(K2, QY2, Y),     X, 4, PX0, 2);  \
    GAP(OY = MFMA32(V0, BF(PY0), OY), X, 8, PX1, 0);  \
    GAP(OY = MFMA32(V1, BF(PY1), OY), X, 12, PX1, 2); } while (0)
#define TILE(KP, VP, K1C, K2C, V0C, V1C, K1N, K2N, V0N, V1N) do { \
    K1N = cat4(lds_b64((KP) + kb1), lds_b64((KP) + kb1b)); K2N = lds_b64((KP) + kb2); V0N = lds_b128((VP) + vb); V1N = lds_b128((VP) + vb + 768); SBAR(); \
    PHASE(sA, pA0, pA1, sB, oB, pB0, pB1, qB1, qB2, K1C, K2C, V0C, V1C); \
    __builtin_amdgcn_s_waitcnt(0xC07F); SBAR(); \
    PHASE(sB, pB0, pB1, sA, oA, pA0, pA1, qA1, qA2, K1N, K2N, V0N, V1N); } while (0)

  STAMP(1);
#pragma unroll
  for (int c = 0; c < 8; ++c) {
    const int slot = c % A_NSLOT, nslot = (c + 1) % A_NSLOT;
    const lds_cptr cp = L + slot * A_CHUNK, np = L + nslot * A_CHUNK;
    const bool mid = c >= 1 && c < 7;
    PRIO(3);
    if (mid) dma1(c + 1, nslot, 1);
    TILE(cp + 1 * 1536, cp + 0 * 1536, k1a, k2a, v0a, v1a, k1b, k2b, v0b, v1b);
    if (mid) dma1(c + 1, nslot, 2);
    TILE(cp + 2 * 1536, cp + 1 * 1536, k1b, k2b, v0b, v1b, k1a, k2a, v0a, v1a);
    PRIO(2);
    WLOAD(c, 0);
    TILE(cp + 3 * 1536, cp + 2 * 1536, k1a, k2a, v0a, v1a, k1b, k2b, v0b, v1b);
    WLOAD(c, 1);
    TILE(cp + 4 * 1536, cp + 3 * 1536, k1b, k2b, v0b, v1b, k1a, k2a, v0a, v1a);
    PRIO(1);
    WLOAD(c, 2);
    TILE(cp + 5 * 1536, cp + 4 * 1536, k1a, k2a, v0a, v1a, k1b, k2b, v0b, v1b);
    WLOAD(c, 3);
    TILE(cp + 6 * 1536, cp + 5 * 1536, k1b, k2b, v0b, v1b, k1a, k2a, v0a, v1a);
    PRIO(0);
    WLOAD(c, 4);
    TILE(cp + 7 * 1536, cp + 6 * 1536, k1a, k2a, v0a, v1a, k1b, k2b, v0b, v1b);
    if (c < 7) WAIT_BAR(0);
    PRIO(3);
    if (c + 2 < 8) dma1(c + 2, (c + 2) % A_NSLOT, 0);
    TILE(np, cp + 7 * 1536, k1b, k2b, v0b, v1b, k1a, k2a, v0a, v1a);
  }
  STAMP(2);
  oB = MFMA32(v0a, BF(pB0), oB);
  oB = MFMA32(v1a, BF(pB1), oB);

  if (__builtin_expect(__any(o_unsafe(oA, h) || o_unsafe(oB, h)), 0)) {
    oA = attn_safe(kg, vg, qA1, q2keepA, r, h);
    oB = attn_safe(kg, vg, qB1, q2keepB, r, h);
  }
  const __amdgpu_buffer_rsrc_t og = __builtin_amdgcn_make_buffer_rsrc(ws + WS_O, 0, 160 * S * 16, 0x00020000);
  attn_store(oA, og, hh, tokA, hh < 32 ? gA : 1.0f - gA, h);
  attn_store(oB, og, hh, tokB, hh < 32 ? gB : 1.0f - gB, h);
  SBAR();

  asm volatile("s_waitcnt vmcnt(0) lgkmcnt(0)\n\ts_barrier" ::: "memory");
  STAMP(3);
  gu32* flags = (gu32*)(ws + WS_F);
  if (wid == 0) {
    if (lane < 8) __hip_atomic_store(flags + (qb * 8 + lane) * 64 + hh, 1u, __ATOMIC_RELAXED, __HIP_MEMORY_SCOPE_AGENT);
    gu32* pf = flags + ((mt >> 3) * 8 + nb) * 64 + lane;
    for (unsigned spins = 0; spins < (1u << 22); ++spins) {
      if (__all(__hip_atomic_load(pf, __ATOMIC_RELAXED, __HIP_MEMORY_SCOPE_AGENT) == 1u)) break;
      __builtin_amdgcn_s_sleep(1);
    }
  }
  asm volatile("s_barrier" ::: "memory");
  STAMP(4);
  const int ob = ((wid * 20 + g) * S + mt * 64 + c16) * 16;
  auto oload = [&](int s) {
#pragma unroll
    for (int m = 0; m < 4; ++m)
      of[s][m] = __builtin_bit_cast(bf16x8, __builtin_amdgcn_raw_buffer_load_b128(og, ob, s * 4 * S * 16 + ((m + wid) & 3) * 256, 16));
  };
#pragma unroll
  for (int s = 0; s < 4; ++s) oload(s);
  SBAR();
  f32x4 acc[4][5] = {};
#pragma unroll
  for (int s = 0; s < 5; ++s) {
#pragma unroll
    for (int m = 3; m >= 0; --m)
#pragma unroll
      for (int n = 0; n < 5; ++n) acc[m][n] = MFMA16(wf[s][n], of[s][m], acc[m][n]);
    if (s < 4) SBAR();
    if (s == 0) { oload(4); SBAR(); }
  }
  const int nh = wid >> 2;
#pragma unroll
  for (int m = 3; m >= 0; --m)
#pragma unroll
    for (int n = 0; n < 5; ++n) {
      const int gm = (m + wid) & 3, owner = gm + (n >= 3 ? 4 : 0), sl = (wid - owner) & 7;
      if (m != 0 || (n >= 3) != (nh == 1)) *(f32x4*)(lds + ((gm * 5 + n) * 7 + sl - 1) * 1024 + lane * 16) = acc[m][n];
    }
  __syncthreads();
  u16* op = out + (size_t)(mt * 64 + (wid & 3) * 16 + c16) * D + nb * 80 + g * 4;
#pragma unroll
  for (int n = 0; n < 5; ++n) {
    if ((n >= 3) == (nh == 1)) {
      f32x4 a = acc[0][n];
#pragma unroll
      for (int sl = 0; sl < 7; ++sl) a += *(const f32x4*)(lds + (((wid & 3) * 5 + n) * 7 + sl) * 1024 + lane * 16);
      *(u32x2*)(op + n * 16) = (u32x2){cvtpk(a[0], a[1]), cvtpk(a[2], a[3])};
    }
  }
  STAMP_OUT(1, ws);
}

extern "C" void kernel_launch(void* const* d_in, const int* in_sizes, int n_in, void* d_out, int out_size, void* d_ws, size_t ws_size,
                              hipStream_t stream) {
  const u16* x = (const u16*)d_in[0];
  const u16* wqkv = (const u16*)d_in[1];
  const u16* woa = (const u16*)d_in[2];
  const u16* wq = (const u16*)d_in[4];
  const u16* wk = (const u16*)d_in[5];
  const u16* wv = (const u16*)d_in[6];
  const u16* wob = (const u16*)d_in[7];
  const u16* gw = (const u16*)d_in[8];
  char* ws = (char*)d_ws;
  k1_proj<<<256, 768, 0, stream>>>(x, wqkv, wq, wk, wv, gw, woa, wob, ws);
  k2_attn<<<256, 512, 0, stream>>>(ws, (u16*)d_out);
}
```

```cpp
#include <hip/hip_runtime.h>
#include <hip/hip_bf16.h>
#include <stdint.h>
#include <type_traits>

typedef __attribute__((ext_vector_type(8))) short bf16x8;
typedef __attribute__((ext_vector_type(4))) short bf16x4;
typedef __attribute__((ext_vector_type(16))) float f32x16;
typedef __attribute__((ext_vector_type(4))) float f32x4;
typedef __attribute__((ext_vector_type(2))) float f32x2;
typedef __attribute__((ext_vector_type(4))) unsigned u32x4;
typedef __attribute__((ext_vector_type(2))) unsigned u32x2;
typedef __attribute__((ext_vector_type(2))) __bf16 bf16x2_t;
typedef __attribute__((address_space(3))) const char* lds_cptr;
typedef unsigned short u16;

#define DI __device__ __forceinline__
#define SBAR() __builtin_amdgcn_sched_barrier(0)
#define PIN(x) asm volatile("" : "+v"(x))
#define MFMA32(a, b, c) __builtin_amdgcn_mfma_f32_32x32x16_bf16((a), (b), (c), 0, 0, 0)
#define MFMA8(a, b, c) __builtin_amdgcn_mfma_f32_32x32x8bf16_1k((a), (b), (c), 0, 0, 0)
#define MFMA16(a, b, c) __builtin_amdgcn_mfma_f32_16x16x32_bf16((a), (b), (c), 0, 0, 0)

constexpr int S = 2048, D = 640;
constexpr float QSCALE = 0.22360679774997896f * 1.4426950408889634f;

constexpr size_t HEAD_B = (size_t)S * 48;
constexpr size_t WS_Q = 0;
constexpr size_t WS_K = WS_Q + 64 * HEAD_B;
constexpr size_t WS_V = WS_K + 33 * HEAD_B;
constexpr size_t WS_G = WS_V + 33 * HEAD_B;
constexpr size_t WS_O = WS_G + S * 4;
constexpr size_t WS_W = WS_O + (size_t)160 * S * 16;
constexpr size_t WS_F = WS_W + (size_t)160 * D * 16;
constexpr size_t WS_END = WS_F + 8192;

DI unsigned cvtpk(float lo, float hi) {
  f32x2 v = {lo, hi};
  return __builtin_bit_cast(unsigned, __builtin_convertvector(v, bf16x2_t));
}
DI void glds16(const void* sbase, unsigned voff, unsigned lds_dst) {
  unsigned keep;
  asm volatile("s_mov_b32 %0, m0\n\ts_mov_b32 m0, %3\n\ts_nop 0\n\tglobal_load_lds_dwordx4 %1, %2\n\ts_mov_b32 m0, %0"
               : "=&s"(keep) : "v"(voff), "s"(sbase), "s"(lds_dst) : "memory");
}
DI bf16x8 lds_b128(lds_cptr p) { return *(const __attribute__((address_space(3))) bf16x8*)p; }
DI bf16x4 lds_b64(lds_cptr p) { return *(const __attribute__((address_space(3))) bf16x4*)p; }
DI bf16x4 lds_tr(lds_cptr p) {
  return __builtin_amdgcn_ds_read_tr16_b64_v4i16((__attribute__((address_space(3))) bf16x4*)p);
}
DI bf16x8 cat4(bf16x4 a, bf16x4 b) { return __builtin_shufflevector(a, b, 0, 1, 2, 3, 4, 5, 6, 7); }
#define WAIT_BAR(N) asm volatile("s_waitcnt vmcnt(" #N ") lgkmcnt(0)\n\ts_barrier" ::: "memory")

constexpr int P_XB = 16384, P_WB = 20480, P_EB = 3072, P_STAGE = P_XB + P_WB + P_EB, P_NST = 4;
constexpr int P_GATE = P_STAGE * P_NST, P_LDS = P_GATE + 2048;

__global__ __launch_bounds__(768, 1) void k1_proj(const u16* __restrict__ x, const u16* __restrict__ wqkv, const u16* __restrict__ wq,
                                                  const u16* __restrict__ wk, const u16* __restrict__ wv, const u16* __restrict__ gw,
                                                  const u16* __restrict__ woa, const u16* __restrict__ wob, char* __restrict__ ws) {
  __shared__ __attribute__((aligned(16))) char lds[P_LDS];
  const int tid = threadIdx.x, lane = tid & 63, r = lane & 31, h = lane >> 5;
  const int wid = __builtin_amdgcn_readfirstlane(tid >> 6);
  const int xcd = blockIdx.x & 7, bi = blockIdx.x >> 3;
  const int mt = (xcd & 1) * 8 + (bi & 7), j = (xcd >> 1) * 4 + (bi >> 3);
  const int m0 = mt * 128;
  const int typ = j >> 2;
  const bool wg_extra = typ == 0 || typ == 3;
  const int ex_rt = j & 3, ex_typ = typ == 3;
  const unsigned lds0 = (unsigned)(uintptr_t)lds;
  const lds_cptr L = (lds_cptr)lds;
  const bool vtype = typ == 2;
  const int rt = wid & 3, grp = wid >> 2;
  const bool my_extra = !vtype && grp == 1 && rt == ex_rt;
  f32x16 acc[3] = {};

  if (grp == 2) {
    unsigned xoff[2], woff[5];
#pragma unroll
    for (int i = 0; i < 2; ++i) {
      const int row = rt * 32 + i * 8 + (lane >> 3), c = (lane & 7) ^ ((row >> 1) & 7);
      xoff[i] = (unsigned)(m0 + row) * 1280u + c * 16;
    }
    auto stage_x = [&](int s) {
#pragma unroll
      for (int i = 0; i < 4; ++i)
        glds16((const char*)x + s * 128 + (i >> 1) * 16 * 1280, xoff[i & 1], lds0 + (s & 3) * P_STAGE + (rt * 4 + i) * 1024);
    };
    stage_x(0);
    const u16* wsrc = (typ < 3) ? (wqkv + j * 160) : (wq + (j - 12) * 160);
    const unsigned ldw = (typ < 3) ? 3840u : 1280u;
    const u16* esrc = ex_typ ? wv : wk;
#pragma unroll
    for (int i = 0; i < 5; ++i) {
      const int idx = i * 64 + lane, row = (idx * 205) >> 12, gr = idx - row * 20;
      woff[i] = (unsigned)(rt * 16 + row) * ldw + gr * 16;
    }
    auto stage_w = [&](int s) {
      const unsigned dst = lds0 + (s & 3) * P_STAGE + P_XB;
      const char* wb = (const char*)wsrc + (size_t)s * 64 * ldw;
#pragma unroll
      for (int i = 0; i < 5; ++i) glds16(wb, woff[i], dst + (rt * 5 + i) * 1024);
      if (wg_extra && rt < 3) {
        if (rt < 2 || lane < 32) glds16((const char*)esrc + s * 2560 + rt * 1024, lane * 16, dst + P_WB + rt * 1024);
      }
    };
    auto stage = [&](int s) { stage_x(s); stage_w(s); };
    stage_w(0);
    if (typ == 1 && rt == 3) {
      glds16(gw, lane * 16, lds0 + P_GATE);
      if (lane < 16) glds16((const char*)gw + 1024, lane * 16, lds0 + P_GATE + 1024);
    }
    stage(1);
#pragma unroll 1
    for (int s = 0; s < 10; ++s) {
      if (s < 9) WAIT_BAR(9); else WAIT_BAR(0);
      if (s + 2 < 10) stage(s + 2);
    }
  } else {
    if (tid < 400) {
      const int task = blockIdx.x * 400 + tid;
      u16 wv8[8];
      const int gr = task / 640, n = task - gr * 640;
      const bool mainp = gr < 128;
      const int hd0 = mainp ? (gr >> 1) : 2 * (gr - 128);
      const u16* w = (hd0 < 32 ? woa + hd0 * 20 * 640 : wob + (hd0 - 32) * 20 * 640) + (mainp ? 4 * (gr & 1) : 16) * 640 + n;
      const int q2 = mainp ? 8 * 640 : 20 * 640;
#pragma unroll
      for (int jj = 0; jj < 4; ++jj) { wv8[jj] = w[jj * 640]; wv8[4 + jj] = w[q2 + jj * 640]; }
      *(u32x4*)(ws + WS_W + (size_t)task * 16) = (u32x4){wv8[0] | ((unsigned)wv8[1] << 16), wv8[2] | ((unsigned)wv8[3] << 16),
                                                         wv8[4] | ((unsigned)wv8[5] << 16), wv8[6] | ((unsigned)wv8[7] << 16)};
    }
    if (wid == 7 && blockIdx.x < 8) *(u32x4*)(ws + WS_F + blockIdx.x * 1024 + lane * 16) = (u32x4){0u, 0u, 0u, 0u};
    const int xsw = h ^ ((r >> 1) & 7), xa = (rt * 32 + r) * 128;
    const int q4 = (lane & 15) >> 2, p4 = lane & 3, b16 = (lane >> 4) & 1;
    const int wbase = P_XB + (8 * h + q4) * 320 + (16 * b16 + 4 * p4) * 2 + grp * 192;
    const int ebase = P_XB + P_WB + (8 * h + q4) * 40 + (16 * b16 + 4 * p4) * 2;
    auto mainloop = [&](auto vt_, auto mode_) {
      constexpr bool VT = decltype(vt_)::value;
      constexpr int MODE = decltype(mode_)::value;
      struct Frag { bf16x8 x, w0, w1, w2; };
      auto rd = [&](Frag& f, int s, int ks) {
        const lds_cptr st = L + (s & 3) * P_STAGE;
        const lds_cptr wp = st + wbase + ks * 16 * 320;
        f.x = lds_b128(st + xa + ((2 * ks) ^ xsw) * 16);
        f.w0 = cat4(lds_tr(wp), lds_tr(wp + 4 * 320));
        f.w1 = cat4(lds_tr(wp + 64), lds_tr(wp + 64 + 4 * 320));
        if constexpr (MODE == 0) f.w2 = cat4(lds_tr(wp + 128), lds_tr(wp + 128 + 4 * 320));
        if constexpr (MODE == 2 || MODE == 3) {
          const lds_cptr ep = st + ebase + ks * 16 * 40;
          f.w2 = cat4(lds_tr(ep), lds_tr(ep + 4 * 40));
        }
        if constexpr (MODE == 4) f.w2 = lds_b128(L + P_GATE + (s * 64 + ks * 16 + 8 * h) * 2);
      };
      auto mm = [&](f32x16& a, const bf16x8& xf, const bf16x8& wf) {
        if constexpr (VT) a = MFMA32(xf, wf, a); else a = MFMA32(wf, xf, a);
      };
      auto mmA = [&](const Frag& f) {
        mm(acc[0], f.x, f.w0); mm(acc[1], f.x, f.w1);
        if constexpr (MODE == 0) mm(acc[2], f.x, f.w2);
        if constexpr (MODE == 2 || MODE == 4) acc[2] = MFMA32(f.w2, f.x, acc[2]);
        if constexpr (MODE == 3) acc[2] = MFMA32(f.x, f.w2, acc[2]);
      };
      auto ilv = [&]() {
#pragma unroll
        for (int i = 0; i < (MODE == 1 ? 2 : 3); ++i) {
          __builtin_amdgcn_sched_group_barrier(0x008, 1, 0);
          __builtin_amdgcn_sched_group_barrier(0x100, 3, 0);
        }
      };
      Frag f0, f1;
      asm volatile("s_barrier" ::: "memory");
      rd(f0, 0, 0);
      SBAR();
#pragma unroll 1
      for (int s = 0; s < 9; ++s) {
        rd(f1, s, 1); mmA(f0); ilv(); SBAR();
        rd(f0, s, 2); mmA(f1); ilv(); SBAR();
        rd(f1, s, 3); mmA(f0); ilv(); SBAR();
        asm volatile("s_waitcnt lgkmcnt(0)\n\ts_barrier" ::: "memory");
        rd(f0, s + 1, 0); mmA(f1); ilv(); SBAR();
      }
      rd(f1, 9, 1); mmA(f0); ilv(); SBAR();
      rd(f0, 9, 2); mmA(f1); ilv(); SBAR();
      rd(f1, 9, 3); mmA(f0); ilv(); SBAR();
      mmA(f1);
    };
    using std::integral_constant;
    if (vtype) {
      if (grp == 0) mainloop(integral_constant<bool, true>{}, integral_constant<int, 0>{});
      else          mainloop(integral_constant<bool, true>{}, integral_constant<int, 1>{});
    } else if (grp == 0) mainloop(integral_constant<bool, false>{}, integral_constant<int, 0>{});
    else if (!my_extra)  mainloop(integral_constant<bool, false>{}, integral_constant<int, 1>{});
    else if (typ == 0)   mainloop(integral_constant<bool, false>{}, integral_constant<int, 2>{});
    else if (typ == 3)   mainloop(integral_constant<bool, false>{}, integral_constant<int, 3>{});
    else                 mainloop(integral_constant<bool, false>{}, integral_constant<int, 4>{});
  }

  const int ntile = grp == 0 ? 3 : 2;
  if (!vtype) {
    char* base = ws + (typ == 1 ? WS_K + (size_t)(j - 4) * 8 * HEAD_B : WS_Q + (size_t)(typ == 0 ? j : j - 8) * 8 * HEAD_B) + (size_t)(m0 + rt * 32) * 48;
    if (grp < 2) {
      const f32x2 sc = {typ == 1 ? 1.0f : QSCALE, typ == 1 ? 1.0f : QSCALE};
#pragma unroll
      for (int t = 0; t < 3; ++t) {
        if (t < ntile) {
          const f32x16& a = acc[t];
#pragma unroll
          for (int g = 0; g < 4; ++g) {
            const int qd = 8 * (grp * 3 + t) + 2 * g + h, hl = (qd * 205) >> 10, dq = qd - hl * 5;
            const unsigned off = (unsigned)hl * (unsigned)HEAD_B + dq * 256 + r * 8;
            const f32x2 lo = (f32x2){a[4 * g], a[4 * g + 1]} * sc, hi = (f32x2){a[4 * g + 2], a[4 * g + 3]} * sc;
            *(u32x2*)(base + off) = (u32x2){cvtpk(lo[0], lo[1]), cvtpk(hi[0], hi[1])};
          }
        }
      }
    } else if (typ == 1) {
#pragma unroll
      for (int i = 0; i < 4; ++i) *(u32x2*)(base + (size_t)(2 * i + h) * HEAD_B + 5 * 256 + r * 8) = (u32x2){0x3F80u, 0u};
    }
  } else {
    char* base = ws + WS_V + (size_t)(j - 8) * 8 * HEAD_B + (size_t)(m0 + rt * 32) * 48;
    if (grp < 2) {
#pragma unroll
      for (int t = 0; t < 3; ++t) {
        if (t < ntile) {
          const f32x16& a = acc[t];
          const int n = 32 * (grp * 3 + t) + r, hl = (n * 205) >> 12, d = n - hl * 20;
          const unsigned off = (unsigned)hl * (unsigned)HEAD_B + h * 384 + d * 16;
#pragma unroll
          for (int sp = 0; sp < 2; ++sp) {
            u32x4 v = {cvtpk(a[8 * sp], a[8 * sp + 1]), cvtpk(a[8 * sp + 2], a[8 * sp + 3]),
                       cvtpk(a[8 * sp + 4], a[8 * sp + 5]), cvtpk(a[8 * sp + 6], a[8 * sp + 7])};
            *(u32x4*)(base + off + sp * 768) = v;
          }
        }
      }
    } else {
      const unsigned cv = (r & 3) == 0 ? 0x3F803F80u : 0u;
#pragma unroll
      for (int sp = 0; sp < 2; ++sp)
        *(u32x4*)(base + (size_t)(r >> 2) * HEAD_B + h * 384 + sp * 768 + (20 + (r & 3)) * 16) = (u32x4){cv, cv, cv, cv};
    }
  }
  if (my_extra) {
    const f32x16& e = acc[2];
    if (typ == 1) {
      if (h == 0) *(float*)(ws + WS_G + (size_t)(m0 + rt * 32 + r) * 4) = 1.0f / (1.0f + __expf(-e[0]));
    } else if (typ == 0) {
      char* base = ws + WS_K + (size_t)32 * HEAD_B + (size_t)(m0 + rt * 32) * 48 + r * 8;
#pragma unroll
      for (int g = 0; g < 3; ++g) {
        u32x2 v = {cvtpk(e[4 * g], e[4 * g + 1]), cvtpk(e[4 * g + 2], e[4 * g + 3])};
        if (g == 2 && h == 1) v = (u32x2){0x3F80u, 0u};
        *(u32x2*)(base + (2 * g + h) * 256) = v;
      }
    } else if (r < 24) {
      char* base = ws + WS_V + (size_t)32 * HEAD_B + (size_t)(m0 + rt * 32) * 48 + h * 384 + r * 16;
      const unsigned cv = r == 20 ? 0x3F803F80u : 0u;
#pragma unroll
      for (int sp = 0; sp < 2; ++sp) {
        u32x4 v = {cvtpk(e[8 * sp], e[8 * sp + 1]), cvtpk(e[8 * sp + 2], e[8 * sp + 3]),
                   cvtpk(e[8 * sp + 4], e[8 * sp + 5]), cvtpk(e[8 * sp + 6], e[8 * sp + 7])};
        if (r >= 20) v = (u32x4){cv, cv, cv, cv};
        *(u32x4*)(base + sp * 768) = v;
      }
    }
  }
}

constexpr int A_CHUNK = 24576, A_KB = 12288, A_NSLOT = 3, A_LDS = 20 * 7 * 1024;

DI float half_max(float m) {
  auto rr = __builtin_amdgcn_permlane32_swap(__float_as_uint(m), __float_as_uint(m), false, false);
  return fmaxf(__uint_as_float(rr[0]), __uint_as_float(rr[1]));
}
DI float max16(const f32x16& s) {
  float a = fmaxf(fmaxf(s[0], s[1]), s[2]), b = fmaxf(fmaxf(s[3], s[4]), s[5]);
  a = fmaxf(fmaxf(a, s[6]), s[7]); b = fmaxf(fmaxf(b, s[8]), s[9]);
  a = fmaxf(fmaxf(a, s[10]), s[11]); b = fmaxf(fmaxf(b, s[12]), s[13]);
  a = fmaxf(fmaxf(a, s[14]), s[15]);
  return half_max(fmaxf(a, b));
}
DI f32x16 attn_safe(const char* kg, const char* vg, bf16x8 q1, bf16x4 q2, int r, int h) {
  if (h) q2 = (bf16x4){0, 0, 0, 0};
  f32x16 o = {};
  float m = -INFINITY;
#pragma unroll 1
  for (int t = 0; t < 64; ++t) {
    const char* kp = kg + t * 1536 + r * 8;
    const bf16x8 k1 = cat4(*(const bf16x4*)(kp + h * 512), *(const bf16x4*)(kp + h * 512 + 256));
    const bf16x4 k2 = *(const bf16x4*)(kp + 1024 + h * 256);
    const int vr = r < 24 ? r : 23;
    const bf16x8 v0 = *(const bf16x8*)(vg + t * 1536 + h * 384 + vr * 16);
    const bf16x8 v1 = *(const bf16x8*)(vg + t * 1536 + 768 + h * 384 + vr * 16);
    f32x16 s = {};
    s = MFMA32(k1, q1, s);
    s = MFMA8(k2, q2, s);
    const float mn = fmaxf(m, max16(s));
    const float al = __builtin_amdgcn_exp2f(m - mn);
    m = mn;
#pragma unroll
    for (int i = 0; i < 16; ++i) { o[i] *= al; s[i] = __builtin_amdgcn_exp2f(s[i] - mn); }
    u32x4 p0 = {cvtpk(s[0], s[1]), cvtpk(s[2], s[3]), cvtpk(s[4], s[5]), cvtpk(s[6], s[7])};
    u32x4 p1 = {cvtpk(s[8], s[9]), cvtpk(s[10], s[11]), cvtpk(s[12], s[13]), cvtpk(s[14], s[15])};
    o = MFMA32(v0, __builtin_bit_cast(bf16x8, p0), o);
    o = MFMA32(v1, __builtin_bit_cast(bf16x8, p1), o);
  }
  return o;
}
DI bool o_unsafe(const f32x16& o, int h) {
  float a = fabsf(o[0]) + fabsf(o[1]), b = fabsf(o[2]) + fabsf(o[3]);
#pragma unroll
  for (int i = 4; i < 8; i += 2) { a += fabsf(o[i]); b += fabsf(o[i + 1]); }
  float c = fabsf(o[8]) + fabsf(o[9]) + fabsf(o[10]) + fabsf(o[11]);
  if (h) c = (o[8] < 1.0e36f && o[8] > 1.0e-25f) ? 0.f : INFINITY;
  return !(a + b + c < 3.0e38f);
}
DI void attn_store(const f32x16& o, __amdgpu_buffer_rsrc_t og, int hh, int tok, float gfac, int h) {
  auto rr = __builtin_amdgcn_permlane32_swap(__float_as_uint(o[8]), __float_as_uint(o[8]), false, false);
  const float sc = gfac * __builtin_amdgcn_rcpf(__uint_as_float(rr[1]));
  u32x4 v = {cvtpk(o[0] * sc, o[1] * sc), cvtpk(o[2] * sc, o[3] * sc), cvtpk(o[4] * sc, o[5] * sc), cvtpk(o[6] * sc, o[7] * sc)};
  __builtin_amdgcn_raw_buffer_store_b128(v, og, ((hh * 2 + h) * S + tok) * 16, 0, 16);
  if (h == 0) {
    u32x2 t = {cvtpk(o[8] * sc, o[9] * sc), cvtpk(o[10] * sc, o[11] * sc)};
    __builtin_amdgcn_raw_buffer_store_b64(t, og, ((128 + (hh >> 1)) * S + tok) * 16 + (hh & 1) * 8, 0, 16);
  }
}
typedef __attribute__((address_space(1))) unsigned gu32;

__global__ __launch_bounds__(512, 1) void k2_attn(char* __restrict__ ws, u16* __restrict__ out) {
  __shared__ __attribute__((aligned(16))) char lds[A_LDS];
  const int tid = threadIdx.x, lane = tid & 63, r = lane & 31, h = lane >> 5;
  const int wid = __builtin_amdgcn_readfirstlane(tid >> 6);
  const int xcd = blockIdx.x & 7, bi = blockIdx.x >> 3;
  const int hh = (bi >> 4) * 32 + xcd * 4 + ((bi >> 2) & 3), qb = bi & 3;
  const int kvh = hh < 32 ? hh : 32;
  const char* kg = ws + WS_K + (size_t)kvh * HEAD_B;
  const char* vg = ws + WS_V + (size_t)kvh * HEAD_B;
  const int tokA = qb * 512 + wid * 64 + r, tokB = tokA + 32;
  const unsigned lds0 = (unsigned)(uintptr_t)lds;
  const lds_cptr L = (lds_cptr)lds;

  const int kb1 = r * 8 + h * 512, kb2 = r * 8 + 1024 + h * 256, vb = A_KB + h * 384 + (r < 21 ? r : 21) * 16;
  bf16x4 qa0, qa1, qb0, qb1, qA2, qB2, ka0, ka1, k2a, k2b;
  {
    const char* qs = ws + WS_Q + (size_t)hh * HEAD_B + (size_t)(qb * 512 + wid * 64) * 48;
    asm volatile("global_load_dwordx2 %0, %9, %12\n\tglobal_load_dwordx2 %1, %9, %12 offset:256\n\tglobal_load_dwordx2 %2, %10, %12\n\t"
                 "global_load_dwordx2 %3, %9, %13\n\tglobal_load_dwordx2 %4, %9, %13 offset:256\n\tglobal_load_dwordx2 %5, %11, %13 offset:1024\n\t"
                 "global_load_dwordx2 %6, %9, %13 offset:1536\n\tglobal_load_dwordx2 %7, %9, %13 offset:1792\n\tglobal_load_dwordx2 %8, %11, %13 offset:2560"
                 : "=&v"(ka0), "=&v"(ka1), "=&v"(k2a), "=&v"(qa0), "=&v"(qa1), "=&v"(qA2), "=&v"(qb0), "=&v"(qb1), "=&v"(qB2)
                 : "v"(kb1), "v"(kb2), "v"(r * 8), "s"(kg), "s"(qs) : "memory");
  }

  const char* dsrc = wid < 4 ? kg + wid * 3072 : vg + (wid - 4) * 3072;
  auto dma1 = [&](int c, int slot, int i) { glds16(dsrc + c * A_KB + i * 1024, lane * 16, lds0 + slot * A_CHUNK + (wid * 3 + i) * 1024); };
  auto dma = [&](int c, int slot) { dma1(c, slot, 0); dma1(c, slot, 1); dma1(c, slot, 2); };
  dma(0, 0);
  asm volatile("s_waitcnt vmcnt(3)" : "+v"(ka0), "+v"(ka1), "+v"(k2a), "+v"(qa0), "+v"(qa1), "+v"(qA2), "+v"(qb0), "+v"(qb1), "+v"(qB2) :: "memory");
  const bf16x8 qA1 = cat4(qa0, qa1), qB1 = cat4(qb0, qb1);
  const int mt = xcd * 4 + (bi & 3), nb = bi >> 2;
  const int c16 = lane & 15, g = lane >> 4;
  bf16x8 wf[5][5], of[5][4];
  const unsigned wvo = (g * D + c16) * 16;
  const char* wgs = ws + WS_W + ((size_t)wid * 20 * D + nb * 80) * 16;
#define WLOAD(c, n) do { if ((c) < 5) asm volatile("global_load_dwordx4 %0, %1, %2 offset:%c3" : "=v"(wf[(c) < 5 ? (c) : 0][n]) \
      : "v"(wvo), "s"(wgs + (c) * (4 * D * 16)), "i"((n) * 256) : "memory"); } while (0)

  int kb1b = kb1 + 256; PIN(kb1b);
  bf16x8 k1a = cat4(ka0, ka1), k1b;
  bf16x8 v0a = {}, v1a = {}, v0b = {}, v1b = {};
  const bf16x4 q2keepA = qA2, q2keepB = qB2;
  f32x16 sA, sB, oA = {}, oB = {};
  const f32x16 zero = {};
  {
    if (h) { qA2 = (bf16x4){0, 0, 0, 0}; qB2 = (bf16x4){0, 0, 0, 0}; }
    sA = MFMA32(k1a, qA1, zero); sB = MFMA32(k1a, qB1, zero);
    sA = MFMA8(k2a, qA2, sA);    sB = MFMA8(k2a, qB2, sB);
    const unsigned ma = cvtpk(-max16(sA), 0.f), mb = cvtpk(-max16(sB), 0.f);
    if (h) { qA2 = __builtin_bit_cast(bf16x4, (u32x2){ma, 0u}); qB2 = __builtin_bit_cast(bf16x4, (u32x2){mb, 0u}); }
    sA = MFMA32(k1a, qA1, zero);
    sA = MFMA8(k2a, qA2, sA);
  }
  u32x4 pA0 = {}, pA1 = {}, pB0 = {}, pB1 = {};
  SBAR();
  WAIT_BAR(0);
  dma(1, 1);
  const float gA = *(const float*)(ws + WS_G + tokA * 4), gB = *(const float*)(ws + WS_G + tokB * 4);

#define PRIO(n) __builtin_amdgcn_s_setprio(n)
#define EX(v) __builtin_amdgcn_exp2f(v)
#define GAP(MF, X, i, P, e) do { MF; SBAR(); X[i] = EX(X[i]); X[i + 1] = EX(X[i + 1]); X[i + 2] = EX(X[i + 2]); X[i + 3] = EX(X[i + 3]); \
    P[e] = cvtpk(X[i], X[i + 1]); P[e + 1] = cvtpk(X[i + 2], X[i + 3]); PIN(P); SBAR(); } while (0)
#define BF(p) __builtin_bit_cast(bf16x8, p)
#define PHASE(X, PX0, PX1, Y, OY, PY0, PY1, QY1, QY2, K1, K2, V0, V1) do { \
    GAP(Y = MFMA32(K1, QY1, zero), X, 0, PX0, 0);  \
    GAP(Y = MFMA8(K2, QY2, Y),     X, 4, PX0, 2);  \
    GAP(OY = MFMA32(V0, BF(PY0), OY), X, 8, PX1, 0);  \
    GAP(OY = MFMA32(V1, BF(PY1), OY), X, 12, PX1, 2); } while (0)
#define TILE(KP, VP, K1C, K2C, V0C, V1C, K1N, K2N, V0N, V1N) do { \
    K1N = cat4(lds_b64((KP) + kb1), lds_b64((KP) + kb1b)); K2N = lds_b64((KP) + kb2); V0N = lds_b128((VP) + vb); V1N = lds_b128((VP) + vb + 768); SBAR(); \
    PHASE(sA, pA0, pA1, sB, oB, pB0, pB1, qB1, qB2, K1C, K2C, V0C, V1C); \
    __builtin_amdgcn_s_waitcnt(0xC07F); SBAR(); \
    PHASE(sB, pB0, pB1, sA, oA, pA0, pA1, qA1, qA2, K1N, K2N, V0N, V1N); } while (0)
#pragma unroll
  for (int c = 0; c < 8; ++c) {
    const int slot = c % A_NSLOT, nslot = (c + 1) % A_NSLOT;
    const lds_cptr cp = L + slot * A_CHUNK, np = L + nslot * A_CHUNK;
    const bool mid = c >= 1 && c < 7;
    PRIO(3);
    if (mid) dma1(c + 1, nslot, 1);
    TILE(cp + 1 * 1536, cp + 0 * 1536, k1a, k2a, v0a, v1a, k1b, k2b, v0b, v1b);
    if (mid) dma1(c + 1, nslot, 2);
    TILE(cp + 2 * 1536, cp + 1 * 1536, k1b, k2b, v0b, v1b, k1a, k2a, v0a, v1a);
    PRIO(2);
    WLOAD(c, 0);
    TILE(cp + 3 * 1536, cp + 2 * 1536, k1a, k2a, v0a, v1a, k1b, k2b, v0b, v1b);
    WLOAD(c, 1);
    TILE(cp + 4 * 1536, cp + 3 * 1536, k1b, k2b, v0b, v1b, k1a, k2a, v0a, v1a);
    PRIO(1);
    WLOAD(c, 2);
    TILE(cp + 5 * 1536, cp + 4 * 1536, k1a, k2a, v0a, v1a, k1b, k2b, v0b, v1b);
    WLOAD(c, 3);
    TILE(cp + 6 * 1536, cp + 5 * 1536, k1b, k2b, v0b, v1b, k1a, k2a, v0a, v1a);
    PRIO(0);
    WLOAD(c, 4);
    TILE(cp + 7 * 1536, cp + 6 * 1536, k1a, k2a, v0a, v1a, k1b, k2b, v0b, v1b);
    if (c < 7) WAIT_BAR(0);
    PRIO(3);
    if (c + 2 < 8) dma1(c + 2, (c + 2) % A_NSLOT, 0);
    TILE(np, cp + 7 * 1536, k1b, k2b, v0b, v1b, k1a, k2a, v0a, v1a);
  }
  oB = MFMA32(v0a, BF(pB0), oB);
  oB = MFMA32(v1a, BF(pB1), oB);

  if (__builtin_expect(__any(o_unsafe(oA, h) || o_unsafe(oB, h)), 0)) {
    oA = attn_safe(kg, vg, qA1, q2keepA, r, h);
    oB = attn_safe(kg, vg, qB1, q2keepB, r, h);
  }
  const __amdgpu_buffer_rsrc_t og = __builtin_amdgcn_make_buffer_rsrc(ws + WS_O, 0, 160 * S * 16, 0x00020000);
  attn_store(oA, og, hh, tokA, hh < 32 ? gA : 1.0f - gA, h);
  attn_store(oB, og, hh, tokB, hh < 32 ? gB : 1.0f - gB, h);
  SBAR();

  asm volatile("s_waitcnt vmcnt(0) lgkmcnt(0)\n\ts_barrier" ::: "memory");
  gu32* flags = (gu32*)(ws + WS_F);
  if (wid == 0) {
    if (lane < 8) __hip_atomic_store(flags + (qb * 8 + lane) * 64 + hh, 1u, __ATOMIC_RELAXED, __HIP_MEMORY_SCOPE_AGENT);
    gu32* pf = flags + ((mt >> 3) * 8 + nb) * 64 + lane;
    for (unsigned spins = 0; spins < (1u << 20); ++spins) {
      if (__all(__hip_atomic_load(pf, __ATOMIC_RELAXED, __HIP_MEMORY_SCOPE_AGENT) == 1u)) break;
      __builtin_amdgcn_s_sleep(1);
    }
  }
  asm volatile("s_barrier" ::: "memory");
  const int ob = ((wid * 20 + g) * S + mt * 64 + c16) * 16;
  auto oload = [&](int s) {
#pragma unroll
    for (int m = 0; m < 4; ++m)
      of[s][m] = __builtin_bit_cast(bf16x8, __builtin_amdgcn_raw_buffer_load_b128(og, ob, s * 4 * S * 16 + ((m + wid) & 3) * 256, 16));
  };
#pragma unroll
  for (int s = 0; s < 5; ++s) oload(s);
  SBAR();
  f32x4 acc[4][5] = {};
#pragma unroll
  for (int s = 0; s < 5; ++s) {
#pragma unroll
    for (int m = 3; m >= 0; --m)
#pragma unroll
      for (int n = 0; n < 5; ++n) acc[m][n] = MFMA16(wf[s][n], of[s][m], acc[m][n]);
    if (s < 4) SBAR();
  }
  const int nh = wid >> 2;
#pragma unroll
  for (int m = 3; m >= 0; --m)
#pragma unroll
    for (int n = 0; n < 5; ++n) {
      const int gm = (m + wid) & 3, owner = gm + (n >= 3 ? 4 : 0), sl = (wid - owner) & 7;
      if (m != 0 || (n >= 3) != (nh == 1)) *(f32x4*)(lds + ((gm * 5 + n) * 7 + sl - 1) * 1024 + lane * 16) = acc[m][n];
    }
  __syncthreads();
  u16* op = out + (size_t)(mt * 64 + (wid & 3) * 16 + c16) * D + nb * 80 + g * 4;
#pragma unroll
  for (int n = 0; n < 5; ++n) {
    if ((n >= 3) == (nh == 1)) {
      f32x4 a = acc[0][n];
#pragma unroll
      for (int sl = 0; sl < 7; ++sl) a += *(const f32x4*)(lds + (((wid & 3) * 5 + n) * 7 + sl) * 1024 + lane * 16);
      *(u32x2*)(op + n * 16) = (u32x2){cvtpk(a[0], a[1]), cvtpk(a[2], a[3])};
    }
  }
}

extern "C" void kernel_launch(void* const* d_in, const int* in_sizes, int n_in, void* d_out, int out_size, void* d_ws, size_t ws_size,
                              hipStream_t stream) {
  const u16* x = (const u16*)d_in[0];
  const u16* wqkv = (const u16*)d_in[1];
  const u16* woa = (const u16*)d_in[2];
  const u16* wq = (const u16*)d_in[4];
  const u16* wk = (const u16*)d_in[5];
  const u16* wv = (const u16*)d_in[6];
  const u16* wob = (const u16*)d_in[7];
  const u16* gw = (const u16*)d_in[8];
  char* ws = (char*)d_ws;
  k1_proj<<<256, 768, 0, stream>>>(x, wqkv, wq, wk, wv, gw, woa, wob, ws);
  k2_attn<<<256, 512, 0, stream>>>(ws, (u16*)d_out);
}
```

```cpp
#include <hip/hip_runtime.h>
#include <hip/hip_bf16.h>
#include <stdint.h>
#include <type_traits>

typedef __attribute__((ext_vector_type(8))) short bf16x8;
typedef __attribute__((ext_vector_type(4))) short bf16x4;
typedef __attribute__((ext_vector_type(16))) float f32x16;
typedef __attribute__((ext_vector_type(4))) float f32x4;
typedef __attribute__((ext_vector_type(2))) float f32x2;
typedef __attribute__((ext_vector_type(4))) unsigned u32x4;
typedef __attribute__((ext_vector_type(2))) unsigned u32x2;
typedef __attribute__((ext_vector_type(2))) __bf16 bf16x2_t;
typedef __attribute__((address_space(3))) const char* lds_cptr;
typedef unsigned short u16;

#define DI __device__ __forceinline__
#define SBAR() __builtin_amdgcn_sched_barrier(0)
#define PIN(x) asm volatile("" : "+v"(x))
#define MFMA32(a, b, c) __builtin_amdgcn_mfma_f32_32x32x16_bf16((a), (b), (c), 0, 0, 0)
#define MFMA8(a, b, c) __builtin_amdgcn_mfma_f32_32x32x8bf16_1k((a), (b), (c), 0, 0, 0)
#define MFMA16(a, b, c) __builtin_amdgcn_mfma_f32_16x16x32_bf16((a), (b), (c), 0, 0, 0)

#define STAMP_DECL
#define STAMP(i)
#define STAMP_OUT(kid, wsb)

constexpr int S = 2048, D = 640;
constexpr float QSCALE = 0.22360679774997896f * 1.4426950408889634f;

constexpr size_t HEAD_B = (size_t)S * 48;
constexpr size_t WS_Q = 0;
constexpr size_t WS_K = WS_Q + 64 * HEAD_B;
constexpr size_t WS_V = WS_K + 33 * HEAD_B;
constexpr size_t WS_G = WS_V + 33 * HEAD_B;
constexpr size_t WS_O = WS_G + S * 4;
constexpr size_t WS_W = WS_O + (size_t)160 * S * 16;
constexpr size_t WS_F = WS_W + (size_t)160 * D * 16;
constexpr size_t WS_END = WS_F + 8192;

#define STMODE 0
#define STG(T, p, v) *(T*)(p) = (v)
DI unsigned cvtpk(float lo, float hi) {
  f32x2 v = {lo, hi};
  return __builtin_bit_cast(unsigned, __builtin_convertvector(v, bf16x2_t));
}
DI void glds16(const void* sbase, unsigned voff, unsigned lds_dst) {
  unsigned keep;
  asm volatile("s_mov_b32 %0, m0\n\ts_mov_b32 m0, %3\n\ts_nop 0\n\tglobal_load_lds_dwordx4 %1, %2\n\ts_mov_b32 m0, %0"
               : "=&s"(keep) : "v"(voff), "s"(sbase), "s"(lds_dst) : "memory");
}
DI bf16x8 lds_b128(lds_cptr p) { return *(const __attribute__((address_space(3))) bf16x8*)p; }
DI bf16x4 lds_b64(lds_cptr p) { return *(const __attribute__((address_space(3))) bf16x4*)p; }
DI bf16x4 lds_tr(lds_cptr p) {
  return __builtin_amdgcn_ds_read_tr16_b64_v4i16((__attribute__((address_space(3))) bf16x4*)p);
}
DI bf16x8 cat4(bf16x4 a, bf16x4 b) { return __builtin_shufflevector(a, b, 0, 1, 2, 3, 4, 5, 6, 7); }
#define WAIT_BAR(N) asm volatile("s_waitcnt vmcnt(" #N ") lgkmcnt(0)\n\ts_barrier" ::: "memory")

constexpr int P_XB = 16384, P_WB = 20480, P_EB = 3072, P_STAGE = P_XB + P_WB + P_EB, P_NST = 4;
constexpr int P_GATE = P_STAGE * P_NST, P_LDS = P_GATE + 2048;

__global__ __launch_bounds__(768, 1) void k1_proj(const u16* __restrict__ x, const u16* __restrict__ wqkv, const u16* __restrict__ wq,
                                                  const u16* __restrict__ wk, const u16* __restrict__ wv, const u16* __restrict__ gw,
                                                  const u16* __restrict__ woa, const u16* __restrict__ wob, char* __restrict__ ws) {
  __shared__ __attribute__((aligned(16))) char lds[P_LDS];
  STAMP_DECL STAMP(0);
  const int tid = threadIdx.x, lane = tid & 63, r = lane & 31, h = lane >> 5;
  const int wid = __builtin_amdgcn_readfirstlane(tid >> 6);
  const int xcd = blockIdx.x & 7, bi = blockIdx.x >> 3;
  const int mt = (xcd & 1) * 8 + (bi & 7), j = (xcd >> 1) * 4 + (bi >> 3);
  const int m0 = mt * 128;
  const int typ = j >> 2;
  const bool wg_extra = typ == 0 || typ == 3;
  const int ex_rt = j & 3, ex_typ = typ == 3;
  const unsigned lds0 = (unsigned)(uintptr_t)lds;
  const lds_cptr L = (lds_cptr)lds;
  const bool vtype = typ == 2;
  const int rt = wid & 3, grp = wid >> 2;
  const bool my_extra = !vtype && grp == 1 && rt == ex_rt;
  f32x16 acc[3] = {};

  if (grp == 2) {
    unsigned xoff[2], woff[5];
#pragma unroll
    for (int i = 0; i < 2; ++i) {
      const int row = rt * 32 + i * 8 + (lane >> 3), c = (lane & 7) ^ ((row >> 1) & 7);
      xoff[i] = (unsigned)(m0 + row) * 1280u + c * 16;
    }
    auto stage_x = [&](int s) {
#pragma unroll
      for (int i = 0; i < 4; ++i)
        glds16((const char*)x + s * 128 + (i >> 1) * 16 * 1280, xoff[i & 1], lds0 + (s & 3) * P_STAGE + (rt * 4 + i) * 1024);
    };
    stage_x(0);
    const u16* wsrc = (typ < 3) ? (wqkv + j * 160) : (wq + (j - 12) * 160);
    const unsigned ldw = (typ < 3) ? 3840u : 1280u;
    const u16* esrc = ex_typ ? wv : wk;
#pragma unroll
    for (int i = 0; i < 5; ++i) {
      const int idx = i * 64 + lane, row = (idx * 205) >> 12, gr = idx - row * 20;
      woff[i] = (unsigned)(rt * 16 + row) * ldw + gr * 16;
    }
    auto stage_w = [&](int s) {
      const unsigned dst = lds0 + (s & 3) * P_STAGE + P_XB;
      const char* wb = (const char*)wsrc + (size_t)s * 64 * ldw;
#pragma unroll
      for (int i = 0; i < 5; ++i) glds16(wb, woff[i], dst + (rt * 5 + i) * 1024);
      if (wg_extra && rt < 3) {
        if (rt < 2 || lane < 32) glds16((const char*)esrc + s * 2560 + rt * 1024, lane * 16, dst + P_WB + rt * 1024);
      }
    };
    auto stage = [&](int s) { stage_x(s); stage_w(s); };
    stage_w(0);
    if (typ == 1 && rt == 3) {
      glds16(gw, lane * 16, lds0 + P_GATE);
      if (lane < 16) glds16((const char*)gw + 1024, lane * 16, lds0 + P_GATE + 1024);
    }
    stage(1);
#pragma unroll 1
    for (int s = 0; s < 10; ++s) {
      if (s < 9) WAIT_BAR(9); else WAIT_BAR(0);
      if (s + 2 < 10) stage(s + 2);
    }
  } else {
    if (tid < 400) {
      const int task = blockIdx.x * 400 + tid;
      u16 wv8[8];
      const int gr = task / 640, n = task - gr * 640;
      const bool mainp = gr < 128;
      const int hd0 = mainp ? (gr >> 1) : 2 * (gr - 128);
      const u16* w = (hd0 < 32 ? woa + hd0 * 20 * 640 : wob + (hd0 - 32) * 20 * 640) + (mainp ? 4 * (gr & 1) : 16) * 640 + n;
      const int q2 = mainp ? 8 * 640 : 20 * 640;
#pragma unroll
      for (int jj = 0; jj < 4; ++jj) { wv8[jj] = w[jj * 640]; wv8[4 + jj] = w[q2 + jj * 640]; }
      const u32x4 wv4 = {wv8[0] | ((unsigned)wv8[1] << 16), wv8[2] | ((unsigned)wv8[3] << 16), wv8[4] | ((unsigned)wv8[5] << 16), wv8[6] | ((unsigned)wv8[7] << 16)};
      *(u32x4*)(ws + WS_W + (size_t)task * 16) = wv4;
    }
    if (wid == 7 && blockIdx.x < 8) *(u32x4*)(ws + WS_F + blockIdx.x * 1024 + lane * 16) = (u32x4){0u, 0u, 0u, 0u};
    const int xsw = h ^ ((r >> 1) & 7), xa = (rt * 32 + r) * 128;
    const int q4 = (lane & 15) >> 2, p4 = lane & 3, b16 = (lane >> 4) & 1;
    const int wbase = P_XB + (8 * h + q4) * 320 + (16 * b16 + 4 * p4) * 2 + grp * 192;
    const int ebase = P_XB + P_WB + (8 * h + q4) * 40 + (16 * b16 + 4 * p4) * 2;
    auto mainloop = [&](auto vt_, auto mode_) {
      constexpr bool VT = decltype(vt_)::value;
      constexpr int MODE = decltype(mode_)::value;
      struct Frag { bf16x8 x, w0, w1, w2; };
      auto rd = [&](Frag& f, int s, int ks) {
        const lds_cptr st = L + (s & 3) * P_STAGE;
        const lds_cptr wp = st + wbase + ks * 16 * 320;
        f.x = lds_b128(st + xa + ((2 * ks) ^ xsw) * 16);
        f.w0 = cat4(lds_tr(wp), lds_tr(wp + 4 * 320));
        f.w1 = cat4(lds_tr(wp + 64), lds_tr(wp + 64 + 4 * 320));
        if constexpr (MODE == 0) f.w2 = cat4(lds_tr(wp + 128), lds_tr(wp + 128 + 4 * 320));
        if constexpr (MODE == 2 || MODE == 3) {
          const lds_cptr ep = st + ebase + ks * 16 * 40;
          f.w2 = cat4(lds_tr(ep), lds_tr(ep + 4 * 40));
        }
        if constexpr (MODE == 4) f.w2 = lds_b128(L + P_GATE + (s * 64 + ks * 16 + 8 * h) * 2);
      };
      auto mm = [&](f32x16& a, const bf16x8& xf, const bf16x8& wf) {
        if constexpr (VT) a = MFMA32(xf, wf, a); else a = MFMA32(wf, xf, a);
      };
      auto mmA = [&](const Frag& f) {
        mm(acc[0], f.x, f.w0); mm(acc[1], f.x, f.w1);
        if constexpr (MODE == 0) mm(acc[2], f.x, f.w2);
        if constexpr (MODE == 2 || MODE == 4) acc[2] = MFMA32(f.w2, f.x, acc[2]);
        if constexpr (MODE == 3) acc[2] = MFMA32(f.x, f.w2, acc[2]);
      };
      auto ilv = [&]() {
#pragma unroll
        for (int i = 0; i < (MODE == 1 ? 2 : 3); ++i) {
          __builtin_amdgcn_sched_group_barrier(0x008, 1, 0);
          __builtin_amdgcn_sched_group_barrier(0x100, 3, 0);
        }
      };
      Frag f0, f1;
      asm volatile("s_barrier" ::: "memory");
      rd(f0, 0, 0);
      SBAR();
#pragma unroll 1
      for (int s = 0; s < 9; ++s) {
        rd(f1, s, 1); mmA(f0); ilv(); SBAR();
        rd(f0, s, 2); mmA(f1); ilv(); SBAR();
        rd(f1, s, 3); mmA(f0); ilv(); SBAR();
        asm volatile("s_waitcnt lgkmcnt(0)\n\ts_barrier" ::: "memory");
        rd(f0, s + 1, 0); mmA(f1); ilv(); SBAR();
      }
      rd(f1, 9, 1); mmA(f0); ilv(); SBAR();
      rd(f0, 9, 2); mmA(f1); ilv(); SBAR();
      rd(f1, 9, 3); mmA(f0); ilv(); SBAR();
      mmA(f1);
    };
    using std::integral_constant;
    if (vtype) {
      if (grp == 0) mainloop(integral_constant<bool, true>{}, integral_constant<int, 0>{});
      else          mainloop(integral_constant<bool, true>{}, integral_constant<int, 1>{});
    } else if (grp == 0) mainloop(integral_constant<bool, false>{}, integral_constant<int, 0>{});
    else if (!my_extra)  mainloop(integral_constant<bool, false>{}, integral_constant<int, 1>{});
    else if (typ == 0)   mainloop(integral_constant<bool, false>{}, integral_constant<int, 2>{});
    else if (typ == 3)   mainloop(integral_constant<bool, false>{}, integral_constant<int, 3>{});
    else                 mainloop(integral_constant<bool, false>{}, integral_constant<int, 4>{});
  }
  STAMP(2);

  const int ntile = grp == 0 ? 3 : 2;
  if (!vtype) {
    char* base = ws + (typ == 1 ? WS_K + (size_t)(j - 4) * 8 * HEAD_B : WS_Q + (size_t)(typ == 0 ? j : j - 8) * 8 * HEAD_B) + (size_t)(m0 + rt * 32) * 48;
    if (grp < 2) {
      const f32x2 sc = {typ == 1 ? 1.0f : QSCALE, typ == 1 ? 1.0f : QSCALE};
#pragma unroll
      for (int t = 0; t < 3; ++t) {
        if (t < ntile) {
          const f32x16& a = acc[t];
#pragma unroll
          for (int g = 0; g < 4; ++g) {
            const int qd = 8 * (grp * 3 + t) + 2 * g + h, hl = (qd * 205) >> 10, dq = qd - hl * 5;
            const unsigned off = (unsigned)hl * (unsigned)HEAD_B + dq * 256 + r * 8;
            const f32x2 lo = (f32x2){a[4 * g], a[4 * g + 1]} * sc, hi = (f32x2){a[4 * g + 2], a[4 * g + 3]} * sc;
            STG(u32x2, (base + off), ((u32x2){cvtpk(lo[0], lo[1]), cvtpk(hi[0], hi[1])}));
          }
        }
      }
    } else if (typ == 1) {
#pragma unroll
      for (int i = 0; i < 4; ++i) STG(u32x2, (base + (size_t)(2 * i + h) * HEAD_B + 5 * 256 + r * 8), ((u32x2){0x3F80u, 0u}));
    }
  } else {
    char* base = ws + WS_V + (size_t)(j - 8) * 8 * HEAD_B + (size_t)(m0 + rt * 32) * 48;
    if (grp < 2) {
#pragma unroll
      for (int t = 0; t < 3; ++t) {
        if (t < ntile) {
          const f32x16& a = acc[t];
          const int n = 32 * (grp * 3 + t) + r, hl = (n * 205) >> 12, d = n - hl * 20;
          const unsigned off = (unsigned)hl * (unsigned)HEAD_B + h * 384 + d * 16;
#pragma unroll
          for (int sp = 0; sp < 2; ++sp) {
            u32x4 v = {cvtpk(a[8 * sp], a[8 * sp + 1]), cvtpk(a[8 * sp + 2], a[8 * sp + 3]),
                       cvtpk(a[8 * sp + 4], a[8 * sp + 5]), cvtpk(a[8 * sp + 6], a[8 * sp + 7])};
            STG(u32x4, (base + off + sp * 768), (v));
          }
        }
      }
    } else {
      const unsigned cv = (r & 3) == 0 ? 0x3F803F80u : 0u;
#pragma unroll
      for (int sp = 0; sp < 2; ++sp)
        STG(u32x4, (base + (size_t)(r >> 2) * HEAD_B + h * 384 + sp * 768 + (20 + (r & 3)) * 16), ((u32x4){cv, cv, cv, cv}));
    }
  }
  if (my_extra) {
    const f32x16& e = acc[2];
    if (typ == 1) {
      if (h == 0) *(float*)(ws + WS_G + (size_t)(m0 + rt * 32 + r) * 4) = 1.0f / (1.0f + __expf(-e[0]));
    } else if (typ == 0) {
      char* base = ws + WS_K + (size_t)32 * HEAD_B + (size_t)(m0 + rt * 32) * 48 + r * 8;
#pragma unroll
      for (int g = 0; g < 3; ++g) {
        u32x2 v = {cvtpk(e[4 * g], e[4 * g + 1]), cvtpk(e[4 * g + 2], e[4 * g + 3])};
        if (g == 2 && h == 1) v = (u32x2){0x3F80u, 0u};
        STG(u32x2, (base + (2 * g + h) * 256), (v));
      }
    } else if (r < 24) {
      char* base = ws + WS_V + (size_t)32 * HEAD_B + (size_t)(m0 + rt * 32) * 48 + h * 384 + r * 16;
      const unsigned cv = r == 20 ? 0x3F803F80u : 0u;
#pragma unroll
      for (int sp = 0; sp < 2; ++sp) {
        u32x4 v = {cvtpk(e[8 * sp], e[8 * sp + 1]), cvtpk(e[8 * sp + 2], e[8 * sp + 3]),
                   cvtpk(e[8 * sp + 4], e[8 * sp + 5]), cvtpk(e[8 * sp + 6], e[8 * sp + 7])};
        if (r >= 20) v = (u32x4){cv, cv, cv, cv};
        STG(u32x4, (base + sp * 768), (v));
      }
    }
  }
  STAMP(3);
  STAMP_OUT(0, ws);
}

constexpr int A_CHUNK = 24576, A_KB = 12288, A_NSLOT = 3, A_LDS = 20 * 7 * 1024;

DI float half_max(float m) {
  auto rr = __builtin_amdgcn_permlane32_swap(__float_as_uint(m), __float_as_uint(m), false, false);
  return fmaxf(__uint_as_float(rr[0]), __uint_as_float(rr[1]));
}
DI float max16(const f32x16& s) {
  float a = fmaxf(fmaxf(s[0], s[1]), s[2]), b = fmaxf(fmaxf(s[3], s[4]), s[5]);
  a = fmaxf(fmaxf(a, s[6]), s[7]); b = fmaxf(fmaxf(b, s[8]), s[9]);
  a = fmaxf(fmaxf(a, s[10]), s[11]); b = fmaxf(fmaxf(b, s[12]), s[13]);
  a = fmaxf(fmaxf(a, s[14]), s[15]);
  return half_max(fmaxf(a, b));
}
DI f32x16 attn_safe(const char* kg, const char* vg, bf16x8 q1, bf16x4 q2, int r, int h) {
  if (h) q2 = (bf16x4){0, 0, 0, 0};
  f32x16 o = {};
  float m = -INFINITY;
#pragma unroll 1
  for (int t = 0; t < 64; ++t) {
    const char* kp = kg + t * 1536 + r * 8;
    const bf16x8 k1 = cat4(*(const bf16x4*)(kp + h * 512), *(const bf16x4*)(kp + h * 512 + 256));
    const bf16x4 k2 = *(const bf16x4*)(kp + 1024 + h * 256);
    const int vr = r < 24 ? r : 23;
    const bf16x8 v0 = *(const bf16x8*)(vg + t * 1536 + h * 384 + vr * 16);
    const bf16x8 v1 = *(const bf16x8*)(vg + t * 1536 + 768 + h * 384 + vr * 16);
    f32x16 s = {};
    s = MFMA32(k1, q1, s);
    s = MFMA8(k2, q2, s);
    const float mn = fmaxf(m, max16(s));
    const float al = __builtin_amdgcn_exp2f(m - mn);
    m = mn;
#pragma unroll
    for (int i = 0; i < 16; ++i) { o[i] *= al; s[i] = __builtin_amdgcn_exp2f(s[i] - mn); }
    u32x4 p0 = {cvtpk(s[0], s[1]), cvtpk(s[2], s[3]), cvtpk(s[4], s[5]), cvtpk(s[6], s[7])};
    u32x4 p1 = {cvtpk(s[8], s[9]), cvtpk(s[10], s[11]), cvtpk(s[12], s[13]), cvtpk(s[14], s[15])};
    o = MFMA32(v0, __builtin_bit_cast(bf16x8, p0), o);
    o = MFMA32(v1, __builtin_bit_cast(bf16x8, p1), o);
  }
  return o;
}
DI bool o_unsafe(const f32x16& o, int h) {
  float a = fabsf(o[0]) + fabsf(o[1]), b = fabsf(o[2]) + fabsf(o[3]);
#pragma unroll
  for (int i = 4; i < 8; i += 2) { a += fabsf(o[i]); b += fabsf(o[i + 1]); }
  float c = fabsf(o[8]) + fabsf(o[9]) + fabsf(o[10]) + fabsf(o[11]);
  if (h) c = (o[8] < 1.0e36f && o[8] > 1.0e-25f) ? 0.f : INFINITY;
  return !(a + b + c < 3.0e38f);
}
DI void attn_store(const f32x16& o, __amdgpu_buffer_rsrc_t og, int hh, int tok, float gfac, int h) {
  auto rr = __builtin_amdgcn_permlane32_swap(__float_as_uint(o[8]), __float_as_uint(o[8]), false, false);
  const float sc = gfac * __builtin_amdgcn_rcpf(__uint_as_float(rr[1]));
  u32x4 v = {cvtpk(o[0] * sc, o[1] * sc), cvtpk(o[2] * sc, o[3] * sc), cvtpk(o[4] * sc, o[5] * sc), cvtpk(o[6] * sc, o[7] * sc)};
  __builtin_amdgcn_raw_buffer_store_b128(v, og, ((hh * 2 + h) * S + tok) * 16, 0, 16);
  if (h == 0) {
    u32x2 t = {cvtpk(o[8] * sc, o[9] * sc), cvtpk(o[10] * sc, o[11] * sc)};
    __builtin_amdgcn_raw_buffer_store_b64(t, og, ((128 + (hh >> 1)) * S + tok) * 16 + (hh & 1) * 8, 0, 16);
  }
}
typedef __attribute__((address_space(1))) unsigned gu32;

__global__ __launch_bounds__(512, 1) void k2_attn(char* __restrict__ ws, u16* __restrict__ out) {
  __shared__ __attribute__((aligned(16))) char lds[A_LDS];
  STAMP_DECL STAMP(0);
  const int tid = threadIdx.x, lane = tid & 63, r = lane & 31, h = lane >> 5;
  const int wid = __builtin_amdgcn_readfirstlane(tid >> 6);
  const int xcd = blockIdx.x & 7, bi = blockIdx.x >> 3;
  const int hh = (bi >> 4) * 32 + xcd * 4 + ((bi >> 2) & 3), qb = bi & 3;
  const int kvh = hh < 32 ? hh : 32;
  const char* kg = ws + WS_K + (size_t)kvh * HEAD_B;
  const char* vg = ws + WS_V + (size_t)kvh * HEAD_B;
  const int tokA = qb * 512 + wid * 64 + r, tokB = tokA + 32;
  const unsigned lds0 = (unsigned)(uintptr_t)lds;
  const lds_cptr L = (lds_cptr)lds;

  const int kb1 = r * 8 + h * 512, kb2 = r * 8 + 1024 + h * 256, vb = A_KB + h * 384 + (r < 21 ? r : 21) * 16;
  bf16x4 qa0, qa1, qb0, qb1, qA2, qB2, ka0, ka1, k2a, k2b;
  {
    const char* qs = ws + WS_Q + (size_t)hh * HEAD_B + (size_t)(qb * 512 + wid * 64) * 48;
    asm volatile("global_load_dwordx2 %0, %9, %12\n\tglobal_load_dwordx2 %1, %9, %12 offset:256\n\tglobal_load_dwordx2 %2, %10, %12\n\t"
                 "global_load_dwordx2 %3, %9, %13\n\tglobal_load_dwordx2 %4, %9, %13 offset:256\n\tglobal_load_dwordx2 %5, %11, %13 offset:1024\n\t"
                 "global_load_dwordx2 %6, %9, %13 offset:1536\n\tglobal_load_dwordx2 %7, %9, %13 offset:1792\n\tglobal_load_dwordx2 %8, %11, %13 offset:2560"
                 : "=&v"(ka0), "=&v"(ka1), "=&v"(k2a), "=&v"(qa0), "=&v"(qa1), "=&v"(qA2), "=&v"(qb0), "=&v"(qb1), "=&v"(qB2)
                 : "v"(kb1), "v"(kb2), "v"(r * 8), "s"(kg), "s"(qs) : "memory");
  }

  const char* dsrc = wid < 4 ? kg + wid * 3072 : vg + (wid - 4) * 3072;
  auto dma1 = [&](int c, int slot, int i) { glds16(dsrc + c * A_KB + i * 1024, lane * 16, lds0 + slot * A_CHUNK + (wid * 3 + i) * 1024); };
  auto dma = [&](int c, int slot) { dma1(c, slot, 0); dma1(c, slot, 1); dma1(c, slot, 2); };
  dma(0, 0);
  asm volatile("s_waitcnt vmcnt(3)" : "+v"(ka0), "+v"(ka1), "+v"(k2a), "+v"(qa0), "+v"(qa1), "+v"(qA2), "+v"(qb0), "+v"(qb1), "+v"(qB2) :: "memory");
  const bf16x8 qA1 = cat4(qa0, qa1), qB1 = cat4(qb0, qb1);
  const int mt = xcd * 4 + (bi & 3), nb = bi >> 2;
  const int c16 = lane & 15, g = lane >> 4;
  bf16x8 wf[5][5], of[5][4];
  const unsigned wvo = (g * D + c16) * 16;
  const char* wgs = ws + WS_W + ((size_t)wid * 20 * D + nb * 80) * 16;
#define WLOAD(c, n) do { if ((c) < 5) asm volatile("global_load_dwordx4 %0, %1, %2 offset:%c3" : "=v"(wf[(c) < 5 ? (c) : 0][n]) \
      : "v"(wvo), "s"(wgs + (c) * (4 * D * 16)), "i"((n) * 256) : "memory"); } while (0)

  int kb1b = kb1 + 256; PIN(kb1b);
  bf16x8 k1a = cat4(ka0, ka1), k1b;
  bf16x8 v0a = {}, v1a = {}, v0b = {}, v1b = {};
  const bf16x4 q2keepA = qA2, q2keepB = qB2;
  f32x16 sA, sB, oA = {}, oB = {};
  const f32x16 zero = {};
  {
    if (h) { qA2 = (bf16x4){0, 0, 0, 0}; qB2 = (bf16x4){0, 0, 0, 0}; }
    sA = MFMA32(k1a, qA1, zero); sB = MFMA32(k1a, qB1, zero);
    sA = MFMA8(k2a, qA2, sA);    sB = MFMA8(k2a, qB2, sB);
    const unsigned ma = cvtpk(-max16(sA), 0.f), mb = cvtpk(-max16(sB), 0.f);
    if (h) { qA2 = __builtin_bit_cast(bf16x4, (u32x2){ma, 0u}); qB2 = __builtin_bit_cast(bf16x4, (u32x2){mb, 0u}); }
    sA = MFMA32(k1a, qA1, zero);
    sA = MFMA8(k2a, qA2, sA);
  }
  u32x4 pA0 = {}, pA1 = {}, pB0 = {}, pB1 = {};
  SBAR();
  WAIT_BAR(0);
  dma(1, 1);
  const float gA = *(const float*)(ws + WS_G + tokA * 4), gB = *(const float*)(ws + WS_G + tokB * 4);

#define PRIO(n) __builtin_amdgcn_s_setprio(n)
#define EX(v) __builtin_amdgcn_exp2f(v)
#define GAP(MF, X, i, P, e) do { MF; SBAR(); X[i] = EX(X[i]); X[i + 1] = EX(X[i + 1]); X[i + 2] = EX(X[i + 2]); X[i + 3] = EX(X[i + 3]); \
    P[e] = cvtpk(X[i], X[i + 1]); P[e + 1] = cvtpk(X[i + 2], X[i + 3]); PIN(P); SBAR(); } while (0)
#define BF(p) __builtin_bit_cast(bf16x8, p)
#define PHASE(X, PX0, PX1, Y, OY, PY0, PY1, QY1, QY2, K1, K2, V0, V1) do { \
    GAP(Y = MFMA32(K1, QY1, zero), X, 0, PX0, 0);  \
    GAP(Y = MFMA8(K2, QY2, Y),     X, 4, PX0, 2);  \
    GAP(OY = MFMA32(V0, BF(PY0), OY), X, 8, PX1, 0);  \
    GAP(OY = MFMA32(V1, BF(PY1), OY), X, 12, PX1, 2); } while (0)
#define TILE(KP, VP, K1C, K2C, V0C, V1C, K1N, K2N, V0N, V1N) do { \
    K1N = cat4(lds_b64((KP) + kb1), lds_b64((KP) + kb1b)); K2N = lds_b64((KP) + kb2); V0N = lds_b128((VP) + vb); V1N = lds_b128((VP) + vb + 768); SBAR(); \
    PHASE(sA, pA0, pA1, sB, oB, pB0, pB1, qB1, qB2, K1C, K2C, V0C, V1C); \
    __builtin_amdgcn_s_waitcnt(0xC07F); SBAR(); \
    PHASE(sB, pB0, pB1, sA, oA, pA0, pA1, qA1, qA2, K1N, K2N, V0N, V1N); } while (0)

  STAMP(1);
#pragma unroll
  for (int c = 0; c < 8; ++c) {
    const int slot = c % A_NSLOT, nslot = (c + 1) % A_NSLOT;
    const lds_cptr cp = L + slot * A_CHUNK, np = L + nslot * A_CHUNK;
    const bool mid = c >= 1 && c < 7;
    PRIO(3);
    if (mid) dma1(c + 1, nslot, 1);
    TILE(cp + 1 * 1536, cp + 0 * 1536, k1a, k2a, v0a, v1a, k1b, k2b, v0b, v1b);
    if (mid) dma1(c + 1, nslot, 2);
    TILE(cp + 2 * 1536, cp + 1 * 1536, k1b, k2b, v0b, v1b, k1a, k2a, v0a, v1a);
    PRIO(2);
    WLOAD(c, 0);
    TILE(cp + 3 * 1536, cp + 2 * 1536, k1a, k2a, v0a, v1a, k1b, k2b, v0b, v1b);
    WLOAD(c, 1);
    TILE(cp + 4 * 1536, cp + 3 * 1536, k1b, k2b, v0b, v1b, k1a, k2a, v0a, v1a);
    PRIO(1);
    WLOAD(c, 2);
    TILE(cp + 5 * 1536, cp + 4 * 1536, k1a, k2a, v0a, v1a, k1b, k2b, v0b, v1b);
    WLOAD(c, 3);
    TILE(cp + 6 * 1536, cp + 5 * 1536, k1b, k2b, v0b, v1b, k1a, k2a, v0a, v1a);
    PRIO(0);
    WLOAD(c, 4);
    TILE(cp + 7 * 1536, cp + 6 * 1536, k1a, k2a, v0a, v1a, k1b, k2b, v0b, v1b);
    if (c < 7) WAIT_BAR(0);
    PRIO(3);
    if (c + 2 < 8) dma1(c + 2, (c + 2) % A_NSLOT, 0);
    TILE(np, cp + 7 * 1536, k1b, k2b, v0b, v1b, k1a, k2a, v0a, v1a);
  }
  STAMP(2);
  oB = MFMA32(v0a, BF(pB0), oB);
  oB = MFMA32(v1a, BF(pB1), oB);

  if (__builtin_expect(__any(o_unsafe(oA, h) || o_unsafe(oB, h)), 0)) {
    oA = attn_safe(kg, vg, qA1, q2keepA, r, h);
    oB = attn_safe(kg, vg, qB1, q2keepB, r, h);
  }
  const __amdgpu_buffer_rsrc_t og = __builtin_amdgcn_make_buffer_rsrc(ws + WS_O, 0, 160 * S * 16, 0x00020000);
  attn_store(oA, og, hh, tokA, hh < 32 ? gA : 1.0f - gA, h);
  attn_store(oB, og, hh, tokB, hh < 32 ? gB : 1.0f - gB, h);
  SBAR();

  asm volatile("s_waitcnt vmcnt(0) lgkmcnt(0)\n\ts_barrier" ::: "memory");
  STAMP(3);
  gu32* flags = (gu32*)(ws + WS_F);
  if (wid == 0) {
    if (lane < 8) __hip_atomic_store(flags + (qb * 8 + lane) * 64 + hh, 1u, __ATOMIC_RELAXED, __HIP_MEMORY_SCOPE_AGENT);
    gu32* pf = flags + ((mt >> 3) * 8 + nb) * 64 + lane;
    for (unsigned spins = 0; spins < (1u << 20); ++spins) {
      if (__all(__hip_atomic_load(pf, __ATOMIC_RELAXED, __HIP_MEMORY_SCOPE_AGENT) == 1u)) break;
      __builtin_amdgcn_s_sleep(1);
    }
  }
  asm volatile("s_barrier" ::: "memory");
  STAMP(4);
  const int ob = ((wid * 20 + g) * S + mt * 64 + c16) * 16;
  auto oload = [&](int s) {
#pragma unroll
    for (int m = 0; m < 4; ++m)
      of[s][m] = __builtin_bit_cast(bf16x8, __builtin_amdgcn_raw_buffer_load_b128(og, ob, s * 4 * S * 16 + ((m + wid) & 3) * 256, 16));
  };
#pragma unroll
  for (int s = 0; s < 5; ++s) oload(s);
  SBAR();
  f32x4 acc[4][5] = {};
#pragma unroll
  for (int s = 0; s < 5; ++s) {
#pragma unroll
    for (int m = 3; m >= 0; --m)
#pragma unroll
      for (int n = 0; n < 5; ++n) acc[m][n] = MFMA16(wf[s][n], of[s][m], acc[m][n]);
    if (s < 4) SBAR();
  }
  const int nh = wid >> 2;
#pragma unroll
  for (int m = 3; m >= 0; --m)
#pragma unroll
    for (int n = 0; n < 5; ++n) {
      const int gm = (m + wid) & 3, owner = gm + (n >= 3 ? 4 : 0), sl = (wid - owner) & 7;
      if (m != 0 || (n >= 3) != (nh == 1)) *(f32x4*)(lds + ((gm * 5 + n) * 7 + sl - 1) * 1024 + lane * 16) = acc[m][n];
    }
  __syncthreads();
  u16* op = out + (size_t)(mt * 64 + (wid & 3) * 16 + c16) * D + nb * 80 + g * 4;
#pragma unroll
  for (int n = 0; n < 5; ++n) {
    if ((n >= 3) == (nh == 1)) {
      f32x4 a = acc[0][n];
#pragma unroll
      for (int sl = 0; sl < 7; ++sl) a += *(const f32x4*)(lds + (((wid & 3) * 5 + n) * 7 + sl) * 1024 + lane * 16);
      *(u32x2*)(op + n * 16) = (u32x2){cvtpk(a[0], a[1]), cvtpk(a[2], a[3])};
    }
  }
  STAMP_OUT(1, ws);
}

extern "C" void kernel_launch(void* const* d_in, const int* in_sizes, int n_in, void* d_out, int out_size, void* d_ws, size_t ws_size,
                              hipStream_t stream) {
  const u16* x = (const u16*)d_in[0];
  const u16* wqkv = (const u16*)d_in[1];
  const u16* woa = (const u16*)d_in[2];
  const u16* wq = (const u16*)d_in[4];
  const u16* wk = (const u16*)d_in[5];
  const u16* wv = (const u16*)d_in[6];
  const u16* wob = (const u16*)d_in[7];
  const u16* gw = (const u16*)d_in[8];
  char* ws = (char*)d_ws;
  k1_proj<<<256, 768, 0, stream>>>(x, wqkv, wq, wk, wv, gw, woa, wob, ws);
  k2_attn<<<256, 512, 0, stream>>>(ws, (u16*)d_out);
}
```

```cpp
#include <hip/hip_runtime.h>
#include <hip/hip_bf16.h>
#include <stdint.h>
#include <type_traits>

typedef __attribute__((ext_vector_type(8))) short bf16x8;
typedef __attribute__((ext_vector_type(4))) short bf16x4;
typedef __attribute__((ext_vector_type(16))) float f32x16;
typedef __attribute__((ext_vector_type(4))) float f32x4;
typedef __attribute__((ext_vector_type(2))) float f32x2;
typedef __attribute__((ext_vector_type(4))) unsigned u32x4;
typedef __attribute__((ext_vector_type(2))) unsigned u32x2;
typedef __attribute__((ext_vector_type(2))) __bf16 bf16x2_t;
typedef __attribute__((address_space(3))) const char* lds_cptr;
typedef unsigned short u16;

#define DI __device__ __forceinline__
#define SBAR() __builtin_amdgcn_sched_barrier(0)
#define PIN(x) asm volatile("" : "+v"(x))
#define MFMA32(a, b, c) __builtin_amdgcn_mfma_f32_32x32x16_bf16((a), (b), (c), 0, 0, 0)
#define MFMA8(a, b, c) __builtin_amdgcn_mfma_f32_32x32x8bf16_1k((a), (b), (c), 0, 0, 0)
#define MFMA16(a, b, c) __builtin_amdgcn_mfma_f32_16x16x32_bf16((a), (b), (c), 0, 0, 0)

constexpr int S = 2048, D = 640;
constexpr float QSCALE = 0.22360679774997896f * 1.4426950408889634f;

constexpr size_t HEAD_B = (size_t)S * 48;
constexpr size_t WS_Q = 0;
constexpr size_t WS_K = WS_Q + 64 * HEAD_B;
constexpr size_t WS_V = WS_K + 33 * HEAD_B;
constexpr size_t WS_G = WS_V + 33 * HEAD_B;
constexpr size_t WS_O = WS_G + S * 4;
constexpr size_t WS_W = WS_O + (size_t)160 * S * 16;
constexpr size_t WS_F = WS_W + (size_t)160 * D * 16;
constexpr size_t WS_END = WS_F + 8192;

DI unsigned cvtpk(float lo, float hi) {
  f32x2 v = {lo, hi};
  return __builtin_bit_cast(unsigned, __builtin_convertvector(v, bf16x2_t));
}
DI void glds16(const void* sbase, unsigned voff, unsigned lds_dst) {
  unsigned keep;
  asm volatile("s_mov_b32 %0, m0\n\ts_mov_b32 m0, %3\n\ts_nop 0\n\tglobal_load_lds_dwordx4 %1, %2\n\ts_mov_b32 m0, %0"
               : "=&s"(keep) : "v"(voff), "s"(sbase), "s"(lds_dst) : "memory");
}
DI bf16x8 lds_b128(lds_cptr p) { return *(const __attribute__((address_space(3))) bf16x8*)p; }
DI bf16x4 lds_b64(lds_cptr p) { return *(const __attribute__((address_space(3))) bf16x4*)p; }
DI bf16x4 lds_tr(lds_cptr p) {
  return __builtin_amdgcn_ds_read_tr16_b64_v4i16((__attribute__((address_space(3))) bf16x4*)p);
}
DI bf16x8 cat4(bf16x4 a, bf16x4 b) { return __builtin_shufflevector(a, b, 0, 1, 2, 3, 4, 5, 6, 7); }
#define WAIT_BAR(N) asm volatile("s_waitcnt vmcnt(" #N ") lgkmcnt(0)\n\ts_barrier" ::: "memory")

constexpr int P_XB = 16384, P_WB = 20480, P_EB = 3072, P_STAGE = P_XB + P_WB + P_EB, P_NST = 4;
constexpr int P_GATE = P_STAGE * P_NST, P_LDS = P_GATE + 2048;

__global__ __launch_bounds__(768, 1) void k1_proj(const u16* __restrict__ x, const u16* __restrict__ wqkv, const u16* __restrict__ wq,
                                                  const u16* __restrict__ wk, const u16* __restrict__ wv, const u16* __restrict__ gw,
                                                  const u16* __restrict__ woa, const u16* __restrict__ wob, char* __restrict__ ws) {
  __shared__ __attribute__((aligned(16))) char lds[P_LDS];
  const int tid = threadIdx.x, lane = tid & 63, r = lane & 31, h = lane >> 5;
  const int wid = __builtin_amdgcn_readfirstlane(tid >> 6);
  const int xcd = blockIdx.x & 7, bi = blockIdx.x >> 3;
  const int mt = (xcd & 1) * 8 + (bi & 7), j = (xcd >> 1) * 4 + (bi >> 3);
  const int m0 = mt * 128;
  const int typ = j >> 2;
  const bool wg_extra = typ == 0 || typ == 3;
  const int ex_rt = j & 3, ex_typ = typ == 3;
  const unsigned lds0 = (unsigned)(uintptr_t)lds;
  const lds_cptr L = (lds_cptr)lds;
  const bool vtype = typ == 2;
  const int rt = wid & 3, grp = wid >> 2;
  const bool my_extra = !vtype && grp == 1 && rt == ex_rt;
  f32x16 acc[3] = {};

  if (grp == 2) {
    unsigned xoff[2], woff[5];
#pragma unroll
    for (int i = 0; i < 2; ++i) {
      const int row = rt * 32 + i * 8 + (lane >> 3), c = (lane & 7) ^ ((row >> 1) & 7);
      xoff[i] = (unsigned)(m0 + row) * 1280u + c * 16;
    }
    auto stage_x = [&](int s) {
#pragma unroll
      for (int i = 0; i < 4; ++i)
        glds16((const char*)x + s * 128 + (i >> 1) * 16 * 1280, xoff[i & 1], lds0 + (s & 3) * P_STAGE + (rt * 4 + i) * 1024);
    };
    stage_x(0);
    const u16* wsrc = (typ < 3) ? (wqkv + j * 160) : (wq + (j - 12) * 160);
    const unsigned ldw = (typ < 3) ? 3840u : 1280u;
    const u16* esrc = ex_typ ? wv : wk;
#pragma unroll
    for (int i = 0; i < 5; ++i) {
      const int idx = i * 64 + lane, row = (idx * 205) >> 12, gr = idx - row * 20;
      woff[i] = (unsigned)(rt * 16 + row) * ldw + gr * 16;
    }
    auto stage_w = [&](int s) {
      const unsigned dst = lds0 + (s & 3) * P_STAGE + P_XB;
      const char* wb = (const char*)wsrc + (size_t)s * 64 * ldw;
#pragma unroll
      for (int i = 0; i < 5; ++i) glds16(wb, woff[i], dst + (rt * 5 + i) * 1024);
      if (wg_extra && rt < 3) {
        if (rt < 2 || lane < 32) glds16((const char*)esrc + s * 2560 + rt * 1024, lane * 16, dst + P_WB + rt * 1024);
      }
    };
    auto stage = [&](int s) { stage_x(s); stage_w(s); };
    stage_w(0);
    if (typ == 1 && rt == 3) {
      glds16(gw, lane * 16, lds0 + P_GATE);
      if (lane < 16) glds16((const char*)gw + 1024, lane * 16, lds0 + P_GATE + 1024);
    }
    stage(1);
#pragma unroll 1
    for (int s = 0; s < 10; ++s) {
      if (s < 9) WAIT_BAR(9); else WAIT_BAR(0);
      if (s + 2 < 10) stage(s + 2);
    }
  } else {
    if (tid < 400) {
      const int task = blockIdx.x * 400 + tid;
      u16 wv8[8];
      const int gr = task / 640, n = task - gr * 640;
      const bool mainp = gr < 128;
      const int hd0 = mainp ? (gr >> 1) : 2 * (gr - 128);
      const u16* w = (hd0 < 32 ? woa + hd0 * 20 * 640 : wob + (hd0 - 32) * 20 * 640) + (mainp ? 4 * (gr & 1) : 16) * 640 + n;
      const int q2 = mainp ? 8 * 640 : 20 * 640;
#pragma unroll
      for (int jj = 0; jj < 4; ++jj) { wv8[jj] = w[jj * 640]; wv8[4 + jj] = w[q2 + jj * 640]; }
      *(u32x4*)(ws + WS_W + (size_t)task * 16) = (u32x4){wv8[0] | ((unsigned)wv8[1] << 16), wv8[2] | ((unsigned)wv8[3] << 16),
                                                         wv8[4] | ((unsigned)wv8[5] << 16), wv8[6] | ((unsigned)wv8[7] << 16)};
    }
    if (wid == 7 && blockIdx.x < 8) *(u32x4*)(ws + WS_F + blockIdx.x * 1024 + lane * 16) = (u32x4){0u, 0u, 0u, 0u};
    const int xsw = h ^ ((r >> 1) & 7), xa = (rt * 32 + r) * 128;
    const int q4 = (lane & 15) >> 2, p4 = lane & 3, b16 = (lane >> 4) & 1;
    const int wbase = P_XB + (8 * h + q4) * 320 + (16 * b16 + 4 * p4) * 2 + grp * 192;
    const int ebase = P_XB + P_WB + (8 * h + q4) * 40 + (16 * b16 + 4 * p4) * 2;
    auto mainloop = [&](auto vt_, auto mode_) {
      constexpr bool VT = decltype(vt_)::value;
      constexpr int MODE = decltype(mode_)::value;
      struct Frag { bf16x8 x, w0, w1, w2; };
      auto rd = [&](Frag& f, int s, int ks) {
        const lds_cptr st = L + (s & 3) * P_STAGE;
        const lds_cptr wp = st + wbase + ks * 16 * 320;
        f.x = lds_b128(st + xa + ((2 * ks) ^ xsw) * 16);
        f.w0 = cat4(lds_tr(wp), lds_tr(wp + 4 * 320));
        f.w1 = cat4(lds_tr(wp + 64), lds_tr(wp + 64 + 4 * 320));
        if constexpr (MODE == 0) f.w2 = cat4(lds_tr(wp + 128), lds_tr(wp + 128 + 4 * 320));
        if constexpr (MODE == 2 || MODE == 3) {
          const lds_cptr ep = st + ebase + ks * 16 * 40;
          f.w2 = cat4(lds_tr(ep), lds_tr(ep + 4 * 40));
        }
        if constexpr (MODE == 4) f.w2 = lds_b128(L + P_GATE + (s * 64 + ks * 16 + 8 * h) * 2);
      };
      auto mm = [&](f32x16& a, const bf16x8& xf, const bf16x8& wf) {
        if constexpr (VT) a = MFMA32(xf, wf, a); else a = MFMA32(wf, xf, a);
      };
      auto mmA = [&](const Frag& f) {
        mm(acc[0], f.x, f.w0); mm(acc[1], f.x, f.w1);
        if constexpr (MODE == 0) mm(acc[2], f.x, f.w2);
        if constexpr (MODE == 2 || MODE == 4) acc[2] = MFMA32(f.w2, f.x, acc[2]);
        if constexpr (MODE == 3) acc[2] = MFMA32(f.x, f.w2, acc[2]);
      };
      auto ilv = [&]() {
#pragma unroll
        for (int i = 0; i < (MODE == 1 ? 2 : 3); ++i) {
          __builtin_amdgcn_sched_group_barrier(0x008, 1, 0);
          __builtin_amdgcn_sched_group_barrier(0x100, 3, 0);
        }
      };
      Frag f0, f1;
      asm volatile("s_barrier" ::: "memory");
      rd(f0, 0, 0);
      SBAR();
#pragma unroll 1
      for (int s = 0; s < 9; ++s) {
        rd(f1, s, 1); mmA(f0); ilv(); SBAR();
        rd(f0, s, 2); mmA(f1); ilv(); SBAR();
        rd(f1, s, 3); mmA(f0); ilv(); SBAR();
        asm volatile("s_waitcnt lgkmcnt(0)\n\ts_barrier" ::: "memory");
        rd(f0, s + 1, 0); mmA(f1); ilv(); SBAR();
      }
      rd(f1, 9, 1); mmA(f0); ilv(); SBAR();
      rd(f0, 9, 2); mmA(f1); ilv(); SBAR();
      rd(f1, 9, 3); mmA(f0); ilv(); SBAR();
      mmA(f1);
    };
    using std::integral_constant;
    if (vtype) {
      if (grp == 0) mainloop(integral_constant<bool, true>{}, integral_constant<int, 0>{});
      else          mainloop(integral_constant<bool, true>{}, integral_constant<int, 1>{});
    } else if (grp == 0) mainloop(integral_constant<bool, false>{}, integral_constant<int, 0>{});
    else if (!my_extra)  mainloop(integral_constant<bool, false>{}, integral_constant<int, 1>{});
    else if (typ == 0)   mainloop(integral_constant<bool, false>{}, integral_constant<int, 2>{});
    else if (typ == 3)   mainloop(integral_constant<bool, false>{}, integral_constant<int, 3>{});
    else                 mainloop(integral_constant<bool, false>{}, integral_constant<int, 4>{});
  }

  const int ntile = grp == 0 ? 3 : 2;
  if (!vtype) {
    char* base = ws + (typ == 1 ? WS_K + (size_t)(j - 4) * 8 * HEAD_B : WS_Q + (size_t)(typ == 0 ? j : j - 8) * 8 * HEAD_B) + (size_t)(m0 + rt * 32) * 48;
    if (grp < 2) {
      const f32x2 sc = {typ == 1 ? 1.0f : QSCALE, typ == 1 ? 1.0f : QSCALE};
#pragma unroll
      for (int t = 0; t < 3; ++t) {
        if (t < ntile) {
          const f32x16& a = acc[t];
#pragma unroll
          for (int g = 0; g < 4; ++g) {
            const int qd = 8 * (grp * 3 + t) + 2 * g + h, hl = (qd * 205) >> 10, dq = qd - hl * 5;
            const unsigned off = (unsigned)hl * (unsigned)HEAD_B + dq * 256 + r * 8;
            const f32x2 lo = (f32x2){a[4 * g], a[4 * g + 1]} * sc, hi = (f32x2){a[4 * g + 2], a[4 * g + 3]} * sc;
            *(u32x2*)(base + off) = (u32x2){cvtpk(lo[0], lo[1]), cvtpk(hi[0], hi[1])};
          }
        }
      }
    } else if (typ == 1) {
#pragma unroll
      for (int i = 0; i < 4; ++i) *(u32x2*)(base + (size_t)(2 * i + h) * HEAD_B + 5 * 256 + r * 8) = (u32x2){0x3F80u, 0u};
    }
  } else {
    char* base = ws + WS_V + (size_t)(j - 8) * 8 * HEAD_B + (size_t)(m0 + rt * 32) * 48;
    if (grp < 2) {
#pragma unroll
      for (int t = 0; t < 3; ++t) {
        if (t < ntile) {
          const f32x16& a = acc[t];
          const int n = 32 * (grp * 3 + t) + r, hl = (n * 205) >> 12, d = n - hl * 20;
          const unsigned off = (unsigned)hl * (unsigned)HEAD_B + h * 384 + d * 16;
#pragma unroll
          for (int sp = 0; sp < 2; ++sp) {
            u32x4 v = {cvtpk(a[8 * sp], a[8 * sp + 1]), cvtpk(a[8 * sp + 2], a[8 * sp + 3]),
                       cvtpk(a[8 * sp + 4], a[8 * sp + 5]), cvtpk(a[8 * sp + 6], a[8 * sp + 7])};
            *(u32x4*)(base + off + sp * 768) = v;
          }
        }
      }
    } else {
      const unsigned cv = (r & 3) == 0 ? 0x3F803F80u : 0u;
#pragma unroll
      for (int sp = 0; sp < 2; ++sp)
        *(u32x4*)(base + (size_t)(r >> 2) * HEAD_B + h * 384 + sp * 768 + (20 + (r & 3)) * 16) = (u32x4){cv, cv, cv, cv};
    }
  }
  if (my_extra) {
    const f32x16& e = acc[2];
    if (typ == 1) {
      if (h == 0) *(float*)(ws + WS_G + (size_t)(m0 + rt * 32 + r) * 4) = 1.0f / (1.0f + __expf(-e[0]));
    } else if (typ == 0) {
      char* base = ws + WS_K + (size_t)32 * HEAD_B + (size_t)(m0 + rt * 32) * 48 + r * 8;
#pragma unroll
      for (int g = 0; g < 3; ++g) {
        u32x2 v = {cvtpk(e[4 * g], e[4 * g + 1]), cvtpk(e[4 * g + 2], e[4 * g + 3])};
        if (g == 2 && h == 1) v = (u32x2){0x3F80u, 0u};
        *(u32x2*)(base + (2 * g + h) * 256) = v;
      }
    } else if (r < 24) {
      char* base = ws + WS_V + (size_t)32 * HEAD_B + (size_t)(m0 + rt * 32) * 48 + h * 384 + r * 16;
      const unsigned cv = r == 20 ? 0x3F803F80u : 0u;
#pragma unroll
      for (int sp = 0; sp < 2; ++sp) {
        u32x4 v = {cvtpk(e[8 * sp], e[8 * sp + 1]), cvtpk(e[8 * sp + 2], e[8 * sp + 3]),
                   cvtpk(e[8 * sp + 4], e[8 * sp + 5]), cvtpk(e[8 * sp + 6], e[8 * sp + 7])};
        if (r >= 20) v = (u32x4){cv, cv, cv, cv};
        *(u32x4*)(base + sp * 768) = v;
      }
    }
  }
}

constexpr int A_CHUNK = 24576, A_KB = 12288, A_NSLOT = 3, A_LDS = 20 * 7 * 1024;

DI float half_max(float m) {
  auto rr = __builtin_amdgcn_permlane32_swap(__float_as_uint(m), __float_as_uint(m), false, false);
  return fmaxf(__uint_as_float(rr[0]), __uint_as_float(rr[1]));
}
DI float max16(const f32x16& s) {
  float a = fmaxf(fmaxf(s[0], s[1]), s[2]), b = fmaxf(fmaxf(s[3], s[4]), s[5]);
  a = fmaxf(fmaxf(a, s[6]), s[7]); b = fmaxf(fmaxf(b, s[8]), s[9]);
  a = fmaxf(fmaxf(a, s[10]), s[11]); b = fmaxf(fmaxf(b, s[12]), s[13]);
  a = fmaxf(fmaxf(a, s[14]), s[15]);
  return half_max(fmaxf(a, b));
}
DI f32x16 attn_safe(const char* kg, const char* vg, bf16x8 q1, bf16x4 q2, int r, int h) {
  if (h) q2 = (bf16x4){0, 0, 0, 0};
  f32x16 o = {};
  float m = -INFINITY;
#pragma unroll 1
  for (int t = 0; t < 64; ++t) {
    const char* kp = kg + t * 1536 + r * 8;
    const bf16x8 k1 = cat4(*(const bf16x4*)(kp + h * 512), *(const bf16x4*)(kp + h * 512 + 256));
    const bf16x4 k2 = *(const bf16x4*)(kp + 1024 + h * 256);
    const int vr = r < 24 ? r : 23;
    const bf16x8 v0 = *(const bf16x8*)(vg + t * 1536 + h * 384 + vr * 16);
    const bf16x8 v1 = *(const bf16x8*)(vg + t * 1536 + 768 + h * 384 + vr * 16);
    f32x16 s = {};
    s = MFMA32(k1, q1, s);
    s = MFMA8(k2, q2, s);
    const float mn = fmaxf(m, max16(s));
    const float al = __builtin_amdgcn_exp2f(m - mn);
    m = mn;
#pragma unroll
    for (int i = 0; i < 16; ++i) { o[i] *= al; s[i] = __builtin_amdgcn_exp2f(s[i] - mn); }
    u32x4 p0 = {cvtpk(s[0], s[1]), cvtpk(s[2], s[3]), cvtpk(s[4], s[5]), cvtpk(s[6], s[7])};
    u32x4 p1 = {cvtpk(s[8], s[9]), cvtpk(s[10], s[11]), cvtpk(s[12], s[13]), cvtpk(s[14], s[15])};
    o = MFMA32(v0, __builtin_bit_cast(bf16x8, p0), o);
    o = MFMA32(v1, __builtin_bit_cast(bf16x8, p1), o);
  }
  return o;
}
DI bool o_unsafe(const f32x16& o, int h) {
  float a = fabsf(o[0]) + fabsf(o[1]), b = fabsf(o[2]) + fabsf(o[3]);
#pragma unroll
  for (int i = 4; i < 8; i += 2) { a += fabsf(o[i]); b += fabsf(o[i + 1]); }
  float c = fabsf(o[8]) + fabsf(o[9]) + fabsf(o[10]) + fabsf(o[11]);
  if (h) c = (o[8] < 1.0e36f && o[8] > 1.0e-25f) ? 0.f : INFINITY;
  return !(a + b + c < 3.0e38f);
}
DI void attn_store(const f32x16& o, __amdgpu_buffer_rsrc_t og, int hh, int tok, float gfac, int h) {
  auto rr = __builtin_amdgcn_permlane32_swap(__float_as_uint(o[8]), __float_as_uint(o[8]), false, false);
  const float sc = gfac * __builtin_amdgcn_rcpf(__uint_as_float(rr[1]));
  u32x4 v = {cvtpk(o[0] * sc, o[1] * sc), cvtpk(o[2] * sc, o[3] * sc), cvtpk(o[4] * sc, o[5] * sc), cvtpk(o[6] * sc, o[7] * sc)};
  __builtin_amdgcn_raw_buffer_store_b128(v, og, ((hh * 2 + h) * S + tok) * 16, 0, 16);
  if (h == 0) {
    u32x2 t = {cvtpk(o[8] * sc, o[9] * sc), cvtpk(o[10] * sc, o[11] * sc)};
    __builtin_amdgcn_raw_buffer_store_b64(t, og, ((128 + (hh >> 1)) * S + tok) * 16 + (hh & 1) * 8, 0, 16);
  }
}
typedef __attribute__((address_space(1))) unsigned gu32;

__global__ __launch_bounds__(512, 1) void k2_attn(char* __restrict__ ws, u16* __restrict__ out) {
  __shared__ __attribute__((aligned(16))) char lds[A_LDS];
  const int tid = threadIdx.x, lane = tid & 63, r = lane & 31, h = lane >> 5;
  const int wid = __builtin_amdgcn_readfirstlane(tid >> 6);
  const int xcd = blockIdx.x & 7, bi = blockIdx.x >> 3;
  const int hh = (bi >> 4) * 32 + xcd * 4 + ((bi >> 2) & 3), qb = bi & 3;
  const int kvh = hh < 32 ? hh : 32;
  const char* kg = ws + WS_K + (size_t)kvh * HEAD_B;
  const char* vg = ws + WS_V + (size_t)kvh * HEAD_B;
  const int tokA = qb * 512 + wid * 64 + r, tokB = tokA + 32;
  const unsigned lds0 = (unsigned)(uintptr_t)lds;
  const lds_cptr L = (lds_cptr)lds;

  const int kb1 = r * 8 + h * 512, kb2 = r * 8 + 1024 + h * 256, vb = A_KB + h * 384 + (r < 21 ? r : 21) * 16;
  bf16x4 qa0, qa1, qb0, qb1, qA2, qB2, ka0, ka1, k2a, k2b;
  {
    const char* qs = ws + WS_Q + (size_t)hh * HEAD_B + (size_t)(qb * 512 + wid * 64) * 48;
    asm volatile("global_load_dwordx2 %0, %9, %12\n\tglobal_load_dwordx2 %1, %9, %12 offset:256\n\tglobal_load_dwordx2 %2, %10, %12\n\t"
                 "global_load_dwordx2 %3, %9, %13\n\tglobal_load_dwordx2 %4, %9, %13 offset:256\n\tglobal_load_dwordx2 %5, %11, %13 offset:1024\n\t"
                 "global_load_dwordx2 %6, %9, %13 offset:1536\n\tglobal_load_dwordx2 %7, %9, %13 offset:1792\n\tglobal_load_dwordx2 %8, %11, %13 offset:2560"
                 : "=&v"(ka0), "=&v"(ka1), "=&v"(k2a), "=&v"(qa0), "=&v"(qa1), "=&v"(qA2), "=&v"(qb0), "=&v"(qb1), "=&v"(qB2)
                 : "v"(kb1), "v"(kb2), "v"(r * 8), "s"(kg), "s"(qs) : "memory");
  }

  const char* dsrc = wid < 4 ? kg + wid * 3072 : vg + (wid - 4) * 3072;
  auto dma1 = [&](int c, int slot, int i) { glds16(dsrc + c * A_KB + i * 1024, lane * 16, lds0 + slot * A_CHUNK + (wid * 3 + i) * 1024); };
  auto dma = [&](int c, int slot) { dma1(c, slot, 0); dma1(c, slot, 1); dma1(c, slot, 2); };
  dma(0, 0);
  asm volatile("s_waitcnt vmcnt(3)" : "+v"(ka0), "+v"(ka1), "+v"(k2a), "+v"(qa0), "+v"(qa1), "+v"(qA2), "+v"(qb0), "+v"(qb1), "+v"(qB2) :: "memory");
  const bf16x8 qA1 = cat4(qa0, qa1), qB1 = cat4(qb0, qb1);
  const int mt = xcd * 4 + (bi & 3), nb = bi >> 2;
  const int c16 = lane & 15, g = lane >> 4;
  bf16x8 wf[5][5], of[5][4];
  const unsigned wvo = (g * D + c16) * 16;
  const char* wgs = ws + WS_W + ((size_t)wid * 20 * D + nb * 80) * 16;
#define WLOAD(c, n) do { if ((c) < 5) asm volatile("global_load_dwordx4 %0, %1, %2 offset:%c3" : "=v"(wf[(c) < 5 ? (c) : 0][n]) \
      : "v"(wvo), "s"(wgs + (c) * (4 * D * 16)), "i"((n) * 256) : "memory"); } while (0)

  int kb1b = kb1 + 256; PIN(kb1b);
  bf16x8 k1a = cat4(ka0, ka1), k1b;
  bf16x8 v0a = {}, v1a = {}, v0b = {}, v1b = {};
  const bf16x4 q2keepA = qA2, q2keepB = qB2;
  f32x16 sA, sB, oA = {}, oB = {};
  const f32x16 zero = {};
  {
    if (h) { qA2 = (bf16x4){0, 0, 0, 0}; qB2 = (bf16x4){0, 0, 0, 0}; }
    sA = MFMA32(k1a, qA1, zero); sB = MFMA32(k1a, qB1, zero);
    sA = MFMA8(k2a, qA2, sA);    sB = MFMA8(k2a, qB2, sB);
    const unsigned ma = cvtpk(-max16(sA), 0.f), mb = cvtpk(-max16(sB), 0.f);
    if (h) { qA2 = __builtin_bit_cast(bf16x4, (u32x2){ma, 0u}); qB2 = __builtin_bit_cast(bf16x4, (u32x2){mb, 0u}); }
    sA = MFMA32(k1a, qA1, zero);
    sA = MFMA8(k2a, qA2, sA);
  }
  u32x4 pA0 = {}, pA1 = {}, pB0 = {}, pB1 = {};
  SBAR();
  WAIT_BAR(0);
  dma(1, 1);
  const float gA = *(const float*)(ws + WS_G + tokA * 4), gB = *(const float*)(ws + WS_G + tokB * 4);

#define PRIO(n) __builtin_amdgcn_s_setprio(n)
#define EX(v) __builtin_amdgcn_exp2f(v)
#define GAP(MF, X, i, P, e) do { MF; SBAR(); X[i] = EX(X[i]); X[i + 1] = EX(X[i + 1]); X[i + 2] = EX(X[i + 2]); X[i + 3] = EX(X[i + 3]); \
    P[e] = PK(X[i], X[i + 1]); P[e + 1] = PK(X[i + 2], X[i + 3]); PIN(P); SBAR(); } while (0)
#define PK(a, b) __builtin_amdgcn_perm(__float_as_uint(b), __float_as_uint(a), 0x07060302u)
#define BF(p) __builtin_bit_cast(bf16x8, p)
#define PHASE(X, PX0, PX1, Y, OY, PY0, PY1, QY1, QY2, K1, K2, V0, V1) do { \
    GAP(Y = MFMA32(K1, QY1, zero), X, 0, PX0, 0);  \
    GAP(Y = MFMA8(K2, QY2, Y),     X, 4, PX0, 2);  \
    GAP(OY = MFMA32(V0, BF(PY0), OY), X, 8, PX1, 0);  \
    GAP(OY = MFMA32(V1, BF(PY1), OY), X, 12, PX1, 2); } while (0)
#define TILE(KP, VP, K1C, K2C, V0C, V1C, K1N, K2N, V0N, V1N) do { \
    K1N = cat4(lds_b64((KP) + kb1), lds_b64((KP) + kb1b)); K2N = lds_b64((KP) + kb2); V0N = lds_b128((VP) + vb); V1N = lds_b128((VP) + vb + 768); SBAR(); \
    PHASE(sA, pA0, pA1, sB, oB, pB0, pB1, qB1, qB2, K1C, K2C, V0C, V1C); \
    __builtin_amdgcn_s_waitcnt(0xC07F); SBAR(); \
    PHASE(sB, pB0, pB1, sA, oA, pA0, pA1, qA1, qA2, K1N, K2N, V0N, V1N); } while (0)
#pragma unroll
  for (int c = 0; c < 8; ++c) {
    const int slot = c % A_NSLOT, nslot = (c + 1) % A_NSLOT;
    const lds_cptr cp = L + slot * A_CHUNK, np = L + nslot * A_CHUNK;
    const bool mid = c >= 1 && c < 7;
    PRIO(3);
    if (mid) dma1(c + 1, nslot, 1);
    TILE(cp + 1 * 1536, cp + 0 * 1536, k1a, k2a, v0a, v1a, k1b, k2b, v0b, v1b);
    if (mid) dma1(c + 1, nslot, 2);
    TILE(cp + 2 * 1536, cp + 1 * 1536, k1b, k2b, v0b, v1b, k1a, k2a, v0a, v1a);
    PRIO(2);
    WLOAD(c, 0);
    TILE(cp + 3 * 1536, cp + 2 * 1536, k1a, k2a, v0a, v1a, k1b, k2b, v0b, v1b);
    WLOAD(c, 1);
    TILE(cp + 4 * 1536, cp + 3 * 1536, k1b, k2b, v0b, v1b, k1a, k2a, v0a, v1a);
    PRIO(1);
    WLOAD(c, 2);
    TILE(cp + 5 * 1536, cp + 4 * 1536, k1a, k2a, v0a, v1a, k1b, k2b, v0b, v1b);
    WLOAD(c, 3);
    TILE(cp + 6 * 1536, cp + 5 * 1536, k1b, k2b, v0b, v1b, k1a, k2a, v0a, v1a);
    PRIO(0);
    WLOAD(c, 4);
    TILE(cp + 7 * 1536, cp + 6 * 1536, k1a, k2a, v0a, v1a, k1b, k2b, v0b, v1b);
    if (c < 7) WAIT_BAR(0);
    PRIO(3);
    if (c + 2 < 8) dma1(c + 2, (c + 2) % A_NSLOT, 0);
    TILE(np, cp + 7 * 1536, k1b, k2b, v0b, v1b, k1a, k2a, v0a, v1a);
  }
  oB = MFMA32(v0a, BF(pB0), oB);
  oB = MFMA32(v1a, BF(pB1), oB);

  if (__builtin_expect(__any(o_unsafe(oA, h) || o_unsafe(oB, h)), 0)) {
    oA = attn_safe(kg, vg, qA1, q2keepA, r, h);
    oB = attn_safe(kg, vg, qB1, q2keepB, r, h);
  }
  const __amdgpu_buffer_rsrc_t og = __builtin_amdgcn_make_buffer_rsrc(ws + WS_O, 0, 160 * S * 16, 0x00020000);
  attn_store(oA, og, hh, tokA, hh < 32 ? gA : 1.0f - gA, h);
  attn_store(oB, og, hh, tokB, hh < 32 ? gB : 1.0f - gB, h);
  SBAR();

  asm volatile("s_waitcnt vmcnt(0) lgkmcnt(0)\n\ts_barrier" ::: "memory");
  gu32* flags = (gu32*)(ws + WS_F);
  if (wid == 0) {
    if (lane < 8) __hip_atomic_store(flags + (qb * 8 + lane) * 64 + hh, 1u, __ATOMIC_RELAXED, __HIP_MEMORY_SCOPE_AGENT);
    gu32* pf = flags + ((mt >> 3) * 8 + nb) * 64 + lane;
    for (unsigned spins = 0; spins < (1u << 20); ++spins) {
      if (__all(__hip_atomic_load(pf, __ATOMIC_RELAXED, __HIP_MEMORY_SCOPE_AGENT) == 1u)) break;
      __builtin_amdgcn_s_sleep(1);
    }
  }
  asm volatile("s_barrier" ::: "memory");
  const int ob = ((wid * 20 + g) * S + mt * 64 + c16) * 16;
  auto oload = [&](int s) {
#pragma unroll
    for (int m = 0; m < 4; ++m)
      of[s][m] = __builtin_bit_cast(bf16x8, __builtin_amdgcn_raw_buffer_load_b128(og, ob, s * 4 * S * 16 + ((m + wid) & 3) * 256, 16));
  };
#pragma unroll
  for (int s = 0; s < 5; ++s) oload(s);
  SBAR();
  f32x4 acc[4][5] = {};
#pragma unroll
  for (int s = 0; s < 5; ++s) {
#pragma unroll
    for (int m = 3; m >= 0; --m)
#pragma unroll
      for (int n = 0; n < 5; ++n) acc[m][n] = MFMA16(wf[s][n], of[s][m], acc[m][n]);
    if (s < 4) SBAR();
  }
  const int nh = wid >> 2;
#pragma unroll
  for (int m = 3; m >= 0; --m)
#pragma unroll
    for (int n = 0; n < 5; ++n) {
      const int gm = (m + wid) & 3, owner = gm + (n >= 3 ? 4 : 0), sl = (wid - owner) & 7;
      if (m != 0 || (n >= 3) != (nh == 1)) *(f32x4*)(lds + ((gm * 5 + n) * 7 + sl - 1) * 1024 + lane * 16) = acc[m][n];
    }
  __syncthreads();
  u16* op = out + (size_t)(mt * 64 + (wid & 3) * 16 + c16) * D + nb * 80 + g * 4;
#pragma unroll
  for (int n = 0; n < 5; ++n) {
    if ((n >= 3) == (nh == 1)) {
      f32x4 a = acc[0][n];
#pragma unroll
      for (int sl = 0; sl < 7; ++sl) a += *(const f32x4*)(lds + (((wid & 3) * 5 + n) * 7 + sl) * 1024 + lane * 16);
      *(u32x2*)(op + n * 16) = (u32x2){cvtpk(a[0], a[1]), cvtpk(a[2], a[3])};
    }
  }
}

extern "C" void kernel_launch(void* const* d_in, const int* in_sizes, int n_in, void* d_out, int out_size, void* d_ws, size_t ws_size,
                              hipStream_t stream) {
  if (n_in < 9 || ws_size < WS_END || out_size != S * D) return;
  const u16* x = (const u16*)d_in[0];
  const u16* wqkv = (const u16*)d_in[1];
  const u16* woa = (const u16*)d_in[2];
  const u16* wq = (const u16*)d_in[4];
  const u16* wk = (const u16*)d_in[5];
  const u16* wv = (const u16*)d_in[6];
  const u16* wob = (const u16*)d_in[7];
  const u16* gw = (const u16*)d_in[8];
  char* ws = (char*)d_ws;
  k1_proj<<<256, 768, 0, stream>>>(x, wqkv, wq, wk, wv, gw, woa, wob, ws);
  k2_attn<<<256, 512, 0, stream>>>(ws, (u16*)d_out);
}
```
